```python
import torch

def _softmax(x, axis=-1):
    return torch.softmax(x, dim=axis)
def _gelu(x):
    return torch.nn.functional.gelu(x, approximate="tanh")
def _silu(x):
    return torch.nn.functional.silu(x)
def _sigmoid(x):
    return torch.sigmoid(x)
def _layernorm(x, axis=-1, eps=1e-5):
    m = x.mean(dim=axis, keepdim=True)
    v = x.var(dim=axis, keepdim=True, unbiased=False)
    return (x - m) / torch.sqrt(v + eps)
def _scatter_add(tgt, idx, src, axis=0):
    return tgt.clone().index_add_(axis, idx, src)
def _segment_sum(src, seg, n):
    out = torch.zeros((n,) + src.shape[1:], dtype=src.dtype)
    return out.index_add_(0, seg, src)
def _pad(x, pw):
    flat = tuple(v for pair in reversed(pw) for v in pair)
    return torch.nn.functional.pad(x, flat)

kernel_name = 'parallel_sum__compressed_kv_cla__glu_gated_swiglu__decode'

def setup_inputs():

    _w = (lambda a: (lambda t: t.to(torch.bfloat16) if t.is_floating_point() else t)(torch.as_tensor(a)))
    def _A_setup():
        B, H, Sq, Sk, D, M = 4, 32, 1, 4096, 128, 576
        Dc = 4 * D
        return {
            "x": _w((lambda *s: torch.randn(*s, dtype=torch.bfloat16) * 0.05)(B, Sq, M)),
            "c_kv": _w((lambda *s: torch.randn(*s, dtype=torch.bfloat16) * 0.8)(B, Sk, Dc)),
            "w_q": _w((lambda *s: torch.randn(*s, dtype=torch.bfloat16) * 1.6)(M, H, D) * (M ** -0.5)),
            "w_uk": _w((lambda *s: torch.randn(*s, dtype=torch.bfloat16) * 1.6)(Dc, H, D) * (Dc ** -0.5)),
            "w_uv": _w((lambda *s: torch.randn(*s, dtype=torch.bfloat16) * 0.8)(Dc, H, D) * (Dc ** -0.5)),
            "w_o": _w((lambda *s: torch.randn(*s, dtype=torch.bfloat16) * 0.8)(H, D, M) * ((H * D) ** -0.5)),
        }
    _A_in = _A_setup()

    def _B_setup():
        B, S, D, F = 4, 1, 576, 4608
        return {
            "x": _w((lambda *s: torch.randn(*s, dtype=torch.bfloat16) * 0.05)(B, S, D)),
            "w1": _w((lambda *s: torch.randn(*s, dtype=torch.bfloat16) * 0.4)(D, F) * (2.0 / D) ** 0.5),
            "w3": _w((lambda *s: torch.randn(*s, dtype=torch.bfloat16) * 0.4)(D, F) * (2.0 / D) ** 0.5),
            "w2": _w((lambda *s: torch.randn(*s, dtype=torch.bfloat16) * 0.4)(F, D) * (2.0 / F) ** 0.5),
        }
    _B_in = _B_setup()

    return {
        "A_x": _A_in["x"],
        "A_c_kv": _A_in["c_kv"],
        "A_w_q": _A_in["w_q"],
        "A_w_uk": _A_in["w_uk"],
        "A_w_uv": _A_in["w_uv"],
        "A_w_o": _A_in["w_o"],
        "B_x": _B_in["x"],
        "B_w1": _B_in["w1"],
        "B_w3": _B_in["w3"],
        "B_w2": _B_in["w2"],
    }

def reference(A_x, A_c_kv, A_w_q, A_w_uk, A_w_uv, A_w_o, B_x, B_w1, B_w3, B_w2):
    def _A_ref(x, c_kv, w_q, w_uk, w_uv, w_o):
        B, Sq, M = x.shape[0], x.shape[1], x.shape[2]
        Sk = c_kv.shape[1]
        H, D = w_q.shape[1], w_q.shape[2]
        q = torch.einsum("bqm,mhd->bhqd", x, w_q)
        k = torch.einsum("bkc,chd->bhkd", c_kv, w_uk)
        v = torch.einsum("bkc,chd->bhkd", c_kv, w_uv)
        scores = torch.einsum("bhqd,bhkd->bhqk", q, k) * (D ** -0.5)
        off = Sk - Sq
        qi = torch.arange(Sq)[:, None]
        kj = torch.arange(Sk)[None, :]
        cmask = kj <= (qi + off)
        scores = torch.where(cmask[None, None], scores, -1e9)
        p = _softmax(scores, axis=-1)
        ctx = torch.einsum("bhqk,bhkd->bhqd", p, v)
        return torch.einsum("bhqd,hdm->bqm", ctx, w_o)

    def _B_ref(x, w1, w3, w2):
        gate = torch.einsum("bsd,df->bsf", x, w1)
        up = torch.einsum("bsd,df->bsf", x, w3)
        h = _silu(gate) * up
        out = torch.einsum("bsf,fd->bsd", h, w2)
        return out

    x = A_x
    xn = _layernorm(x, axis=-1)
    return x + _A_ref(xn, A_c_kv, A_w_q, A_w_uk, A_w_uv, A_w_o) + _B_ref(xn, B_w1, B_w3, B_w2)
```

```cpp
#include <hip/hip_runtime.h>
#include <hip/hip_bf16.h>
#include <stdint.h>

typedef unsigned short u16;
__device__ __forceinline__ float bf2f(u16 v) { return __uint_as_float(((unsigned)v) << 16); }
__device__ __forceinline__ u16 f2bf(float f) { unsigned u = __float_as_uint(f); u += 0x7FFFu + ((u >> 16) & 1u); return (u16)(u >> 16); }
__device__ __forceinline__ float rbf(float f) { return bf2f(f2bf(f)); }

constexpr int B = 4, H = 32, SK = 4096, D = 128, M = 576, DC = 512, F = 4608;

constexpr size_t O_XN = 0;
constexpr size_t O_Q = O_XN + B * M;
constexpr size_t O_HH = O_Q + B * H * D;
constexpr size_t O_QL = O_HH + B * F;
constexpr size_t O_S = O_QL + B * H * DC;
constexpr size_t O_CL = O_S + (size_t)B * H * SK;
constexpr size_t O_CTX = O_CL + B * H * DC;

__global__ void k_ln(const u16* x, float* ws) {
  int b = blockIdx.x; int t = threadIdx.x;
  float v[9]; float s = 0;
  for (int i = 0; i < 9; i++) { v[i] = bf2f(x[b * M + t + 64 * i]); s += v[i]; }
  for (int o = 32; o; o >>= 1) s += __shfl_xor(s, o);
  float meanf = s / M; float mean = rbf(meanf);
  float q = 0; for (int i = 0; i < 9; i++) { float d = v[i] - meanf; q += d * d; }
  for (int o = 32; o; o >>= 1) q += __shfl_xor(q, o);
  float var = rbf(q / M);
  float den = rbf(sqrtf(rbf(var + 1e-5f)));
  for (int i = 0; i < 9; i++) ws[O_XN + b * M + t + 64 * i] = rbf(rbf(v[i] - mean) / den);
}
__global__ void k_q(const u16* wq, const u16* w1, const u16* w3, float* ws) {
  int c = blockIdx.x * 64 + threadIdx.x;
  if (c < H * D) {
    float a[4] = {0, 0, 0, 0};
    for (int m = 0; m < M; m++) { float w = bf2f(wq[(size_t)m * H * D + c]); for (int b = 0; b < 4; b++) a[b] += ws[O_XN + b * M + m] * w; }
    for (int b = 0; b < 4; b++) ws[O_Q + b * H * D + c] = rbf(a[b]);
  } else {
    c -= H * D;
    float g[4] = {0, 0, 0, 0}, u[4] = {0, 0, 0, 0};
    for (int m = 0; m < M; m++) { float wa = bf2f(w1[(size_t)m * F + c]), wb = bf2f(w3[(size_t)m * F + c]); for (int b = 0; b < 4; b++) { float xv = ws[O_XN + b * M + m]; g[b] += xv * wa; u[b] += xv * wb; } }
    for (int b = 0; b < 4; b++) { float gg = rbf(g[b]), uu = rbf(u[b]); float sl = rbf(gg / (1.f + __expf(-gg))); ws[O_HH + b * F + c] = rbf(sl * uu); }
  }
}
__global__ void k_ql(const u16* wuk, float* ws) {
  int idx = blockIdx.x * 64 + threadIdx.x;
  int h = idx / DC, c = idx % DC;
  float a[4] = {0, 0, 0, 0};
  for (int d = 0; d < D; d++) { float w = bf2f(wuk[((size_t)c * H + h) * D + d]); for (int b = 0; b < 4; b++) a[b] += ws[O_Q + b * H * D + h * D + d] * w; }
  for (int b = 0; b < 4; b++) ws[O_QL + (b * H + h) * DC + c] = a[b];
}
__global__ void k_s(const u16* ckv, float* ws) {
  int b = blockIdx.y; int k = blockIdx.x * 64 + threadIdx.x; int h = blockIdx.z;
  float a = 0;
  for (int c = 0; c < DC; c++) a += ws[O_QL + (b * H + h) * DC + c] * bf2f(ckv[((size_t)b * SK + k) * DC + c]);
  ws[O_S + ((size_t)b * H + h) * SK + k] = a * 0.08838834764831845f;
}
__global__ void k_sm(float* ws) {
  int bh = blockIdx.x; int t = threadIdx.x;
  float* s = ws + O_S + (size_t)bh * SK;
  float m = -1e30f; for (int k = t; k < SK; k += 64) m = fmaxf(m, s[k]);
  for (int o = 32; o; o >>= 1) m = fmaxf(m, __shfl_xor(m, o));
  float l = 0; for (int k = t; k < SK; k += 64) { float e = __expf(s[k] - m); s[k] = e; l += e; }
  for (int o = 32; o; o >>= 1) l += __shfl_xor(l, o);
  for (int k = t; k < SK; k += 64) s[k] = s[k] / l;
}
__global__ void k_pv(const u16* ckv, float* ws) {
  int b = blockIdx.y, h = blockIdx.z; int c = blockIdx.x * 64 + threadIdx.x;
  float a = 0; const float* p = ws + O_S + ((size_t)b * H + h) * SK;
  for (int k = 0; k < SK; k++) a += p[k] * bf2f(ckv[((size_t)b * SK + k) * DC + c]);
  ws[O_CL + (b * H + h) * DC + c] = a;
}
__global__ void k_uv(const u16* wuv, float* ws) {
  int idx = blockIdx.x * 64 + threadIdx.x; int h = idx / D;
  float a[4] = {0, 0, 0, 0};
  for (int c = 0; c < DC; c++) { float w = bf2f(wuv[(size_t)c * H * D + idx]); for (int b = 0; b < 4; b++) a[b] += ws[O_CL + (b * H + h) * DC + c] * w; }
  for (int b = 0; b < 4; b++) ws[O_CTX + b * H * D + idx] = rbf(a[b]);
}
__global__ void k_o(const u16* x, const u16* wo, const u16* w2, const float* ws, u16* out) {
  int m = blockIdx.x * 64 + threadIdx.x; int b = blockIdx.y;
  float a = 0, f = 0;
  for (int i = 0; i < H * D; i++) a += ws[O_CTX + b * H * D + i] * bf2f(wo[(size_t)i * M + m]);
  for (int i = 0; i < F; i++) f += ws[O_HH + b * F + i] * bf2f(w2[(size_t)i * M + m]);
  float r = rbf(bf2f(x[b * M + m]) + rbf(a));
  out[b * M + m] = f2bf(r + rbf(f));
}

extern "C" void kernel_launch(void* const* d_in, const int* in_sizes, int n_in, void* d_out, int out_size, void* d_ws, size_t ws_size, hipStream_t stream) {
  const u16* x = (const u16*)d_in[0]; const u16* ckv = (const u16*)d_in[1]; const u16* wq = (const u16*)d_in[2];
  const u16* wuk = (const u16*)d_in[3]; const u16* wuv = (const u16*)d_in[4]; const u16* wo = (const u16*)d_in[5];
  const u16* w1 = (const u16*)d_in[7]; const u16* w3 = (const u16*)d_in[8]; const u16* w2 = (const u16*)d_in[9];
  float* ws = (float*)d_ws; u16* out = (u16*)d_out;
  k_ln<<<4, 64, 0, stream>>>(x, ws);
  k_q<<<(H * D + F) / 64, 64, 0, stream>>>(wq, w1, w3, ws);
  k_ql<<<H * DC / 64, 64, 0, stream>>>(wuk, ws);
  k_s<<<dim3(SK / 64, B, H), 64, 0, stream>>>(ckv, ws);
  k_sm<<<B * H, 64, 0, stream>>>(ws);
  k_pv<<<dim3(DC / 64, B, H), 64, 0, stream>>>(ckv, ws);
  k_uv<<<H * D / 64, 64, 0, stream>>>(wuv, ws);
  k_o<<<dim3(M / 64, B), 64, 0, stream>>>(x, wo, w2, ws, out);
}
```

```cpp
#include <hip/hip_runtime.h>
#include <hip/hip_bf16.h>
#include <stdint.h>

typedef unsigned short u16;
__device__ __forceinline__ float bf2f(u16 v) { return __uint_as_float(((unsigned)v) << 16); }
__device__ __forceinline__ u16 f2bf(float f) { unsigned u = __float_as_uint(f); u += 0x7FFFu + ((u >> 16) & 1u); return (u16)(u >> 16); }
__device__ __forceinline__ float rbf(float f) { return bf2f(f2bf(f)); }

constexpr int B = 4, H = 32, SK = 4096, D = 128, M = 576, DC = 512, F = 4608;

constexpr size_t O_XN = 0;
constexpr size_t O_Q = O_XN + B * M;
constexpr size_t O_HH = O_Q + B * H * D;
constexpr size_t O_QL = O_HH + B * F;
constexpr size_t O_S = O_QL + B * H * DC;
constexpr size_t O_CL = O_S + (size_t)B * H * SK;
constexpr size_t O_CTX = O_CL + B * H * DC;

__global__ void k_ln(const u16* x, float* ws) {
  int b = blockIdx.x; int t = threadIdx.x;
  float v[9]; float s = 0;
  for (int i = 0; i < 9; i++) { v[i] = bf2f(x[b * M + t + 64 * i]); s += v[i]; }
  for (int o = 32; o; o >>= 1) s += __shfl_xor(s, o);
  float meanf = s / M; float mean = rbf(meanf);
  float q = 0; for (int i = 0; i < 9; i++) { float d = v[i] - meanf; q += d * d; }
  for (int o = 32; o; o >>= 1) q += __shfl_xor(q, o);
  float var = rbf(q / M);
  float den = rbf(sqrtf(rbf(var + 1e-5f)));
  for (int i = 0; i < 9; i++) ws[O_XN + b * M + t + 64 * i] = rbf(rbf(v[i] - mean) / den);
}
__global__ void k_q(const u16* wq, const u16* w1, const u16* w3, float* ws) {
  int c = blockIdx.x * 64 + threadIdx.x;
  if (c < H * D) {
    float a[4] = {0, 0, 0, 0};
    for (int m = 0; m < M; m++) { float w = bf2f(wq[(size_t)m * H * D + c]); for (int b = 0; b < 4; b++) a[b] += ws[O_XN + b * M + m] * w; }
    for (int b = 0; b < 4; b++) ws[O_Q + b * H * D + c] = rbf(a[b]);
  } else {
    c -= H * D;
    float g[4] = {0, 0, 0, 0}, u[4] = {0, 0, 0, 0};
    for (int m = 0; m < M; m++) { float wa = bf2f(w1[(size_t)m * F + c]), wb = bf2f(w3[(size_t)m * F + c]); for (int b = 0; b < 4; b++) { float xv = ws[O_XN + b * M + m]; g[b] += xv * wa; u[b] += xv * wb; } }
    for (int b = 0; b < 4; b++) { float gg = rbf(g[b]), uu = rbf(u[b]); float sl = rbf(gg / (1.f + __expf(-gg))); ws[O_HH + b * F + c] = rbf(sl * uu); }
  }
}
__global__ void k_ql(const u16* wuk, float* ws) {
  int idx = blockIdx.x * 64 + threadIdx.x;
  int h = idx / DC, c = idx % DC;
  float a[4] = {0, 0, 0, 0};
  for (int d = 0; d < D; d++) { float w = bf2f(wuk[((size_t)c * H + h) * D + d]); for (int b = 0; b < 4; b++) a[b] += ws[O_Q + b * H * D + h * D + d] * w; }
  for (int b = 0; b < 4; b++) ws[O_QL + (b * H + h) * DC + c] = a[b];
}
__global__ void k_s(const u16* ckv, float* ws) {
  int b = blockIdx.y; int k = blockIdx.x * 64 + threadIdx.x; int h = blockIdx.z;
  float a = 0;
  for (int c = 0; c < DC; c++) a += ws[O_QL + (b * H + h) * DC + c] * bf2f(ckv[((size_t)b * SK + k) * DC + c]);
  ws[O_S + ((size_t)b * H + h) * SK + k] = a * 0.08838834764831845f;
}
__global__ void k_sm(float* ws) {
  int bh = blockIdx.x; int t = threadIdx.x;
  float* s = ws + O_S + (size_t)bh * SK;
  float m = -1e30f; for (int k = t; k < SK; k += 64) m = fmaxf(m, s[k]);
  for (int o = 32; o; o >>= 1) m = fmaxf(m, __shfl_xor(m, o));
  float l = 0; for (int k = t; k < SK; k += 64) { float e = __expf(s[k] - m); s[k] = e; l += e; }
  for (int o = 32; o; o >>= 1) l += __shfl_xor(l, o);
  for (int k = t; k < SK; k += 64) s[k] = s[k] / l;
}
__global__ void k_pv(const u16* ckv, float* ws) {
  int b = blockIdx.y, h = blockIdx.z; int c = blockIdx.x * 64 + threadIdx.x;
  float a = 0; const float* p = ws + O_S + ((size_t)b * H + h) * SK;
  for (int k = 0; k < SK; k++) a += p[k] * bf2f(ckv[((size_t)b * SK + k) * DC + c]);
  ws[O_CL + (b * H + h) * DC + c] = a;
}
__global__ void k_uv(const u16* wuv, float* ws) {
  int idx = blockIdx.x * 64 + threadIdx.x; int h = idx / D;
  float a[4] = {0, 0, 0, 0};
  for (int c = 0; c < DC; c++) { float w = bf2f(wuv[(size_t)c * H * D + idx]); for (int b = 0; b < 4; b++) a[b] += ws[O_CL + (b * H + h) * DC + c] * w; }
  for (int b = 0; b < 4; b++) ws[O_CTX + b * H * D + idx] = rbf(a[b]);
}
__global__ void k_o(const u16* x, const u16* wo, const u16* w2, const float* ws, u16* out) {
  int m = blockIdx.x * 64 + threadIdx.x; int b = blockIdx.y;
  float a = 0, f = 0;
  for (int i = 0; i < H * D; i++) a += ws[O_CTX + b * H * D + i] * bf2f(wo[(size_t)i * M + m]);
  for (int i = 0; i < F; i++) f += ws[O_HH + b * F + i] * bf2f(w2[(size_t)i * M + m]);
  float r = rbf(bf2f(x[b * M + m]) + rbf(a));
  out[b * M + m] = f2bf(r + rbf(f));
}

#define NT 1
#define KAMP 100
#define MASK 30

typedef unsigned v4u __attribute__((ext_vector_type(4)));
typedef unsigned long long u64;
struct SP { const v4u* p[8]; unsigned n[8]; u64* st; };
__global__ void __launch_bounds__(512, 2) k_stream(SP p) {
  u64 t0 = __builtin_amdgcn_s_memrealtime();
  v4u acc = {0,0,0,0};
  int t = threadIdx.x;
  #pragma unroll
  for (int a = 0; a < 8; a++) {
    if (!((MASK >> a) & 1)) continue;
    unsigned per = p.n[a] / 256;
    const v4u* q = p.p[a] + (size_t)blockIdx.x * per;
    v4u v[8];
    #pragma unroll
    for (int k = 0; k < 8; k++) { unsigned i = t + 512 * k; v[k] = v4u{0,0,0,0}; if (i < per) v[k] = NT ? __builtin_nontemporal_load(q + i) : q[i]; }
    #pragma unroll
    for (int k = 0; k < 8; k++) acc ^= v[k];
  }
  unsigned r = acc.x ^ acc.y ^ acc.z ^ acc.w;
  asm volatile("" :: "v"(r));
  __syncthreads();
  u64 t1 = __builtin_amdgcn_s_memrealtime();
  if (t == 0) { p.st[blockIdx.x * 2] = t0; p.st[blockIdx.x * 2 + 1] = t1; }
}
__global__ void k_spin(const u64* st) {
  u64 mn = ~0ull, mx = 0;
  for (int i = 0; i < 256; i++) { u64 a = st[2 * i], b = st[2 * i + 1]; mn = a < mn ? a : mn; mx = b > mx ? b : mx; }
  u64 T = mx - mn; if (T > 5000) T = 5000;
  u64 t0 = __builtin_amdgcn_s_memrealtime();
  while (__builtin_amdgcn_s_memrealtime() < t0 + T * KAMP) __builtin_amdgcn_s_sleep(4);
}
extern "C" void kernel_launch(void* const* d_in, const int* in_sizes, int n_in, void* d_out, int out_size, void* d_ws, size_t ws_size, hipStream_t stream) {
  const u16* x = (const u16*)d_in[0]; const u16* ckv = (const u16*)d_in[1]; const u16* wq = (const u16*)d_in[2];
  const u16* wuk = (const u16*)d_in[3]; const u16* wuv = (const u16*)d_in[4]; const u16* wo = (const u16*)d_in[5];
  const u16* w1 = (const u16*)d_in[7]; const u16* w3 = (const u16*)d_in[8]; const u16* w2 = (const u16*)d_in[9];
  float* ws = (float*)d_ws; u16* out = (u16*)d_out;

  { SP p; int idx[8] = {1,2,3,4,5,7,8,9};
    for (int i = 0; i < 8; i++) { p.p[i] = (const v4u*)d_in[idx[i]]; p.n[i] = in_sizes[idx[i]] / 8; }
    p.st = (u64*)((char*)d_ws + (64 << 20));
    k_stream<<<256, 512, 0, stream>>>(p);
    k_spin<<<1, 64, 0, stream>>>(p.st); }
  k_ln<<<4, 64, 0, stream>>>(x, ws);
  k_q<<<(H * D + F) / 64, 64, 0, stream>>>(wq, w1, w3, ws);
  k_ql<<<H * DC / 64, 64, 0, stream>>>(wuk, ws);
  k_s<<<dim3(SK / 64, B, H), 64, 0, stream>>>(ckv, ws);
  k_sm<<<B * H, 64, 0, stream>>>(ws);
  k_pv<<<dim3(DC / 64, B, H), 64, 0, stream>>>(ckv, ws);
  k_uv<<<H * D / 64, 64, 0, stream>>>(wuv, ws);
  k_o<<<dim3(M / 64, B), 64, 0, stream>>>(x, wo, w2, ws, out);
}
```

```cpp
#include <hip/hip_runtime.h>
#include <hip/hip_bf16.h>
#include <stdint.h>

typedef unsigned short u16;
__device__ __forceinline__ float bf2f(u16 v) { return __uint_as_float(((unsigned)v) << 16); }
__device__ __forceinline__ u16 f2bf(float f) { unsigned u = __float_as_uint(f); u += 0x7FFFu + ((u >> 16) & 1u); return (u16)(u >> 16); }
__device__ __forceinline__ float rbf(float f) { return bf2f(f2bf(f)); }

constexpr int B = 4, H = 32, SK = 4096, D = 128, M = 576, DC = 512, F = 4608;

constexpr size_t O_XN = 0;
constexpr size_t O_Q = O_XN + B * M;
constexpr size_t O_HH = O_Q + B * H * D;
constexpr size_t O_QL = O_HH + B * F;
constexpr size_t O_S = O_QL + B * H * DC;
constexpr size_t O_CL = O_S + (size_t)B * H * SK;
constexpr size_t O_CTX = O_CL + B * H * DC;

__global__ void k_ln(const u16* x, float* ws) {
  int b = blockIdx.x; int t = threadIdx.x;
  float v[9]; float s = 0;
  for (int i = 0; i < 9; i++) { v[i] = bf2f(x[b * M + t + 64 * i]); s += v[i]; }
  for (int o = 32; o; o >>= 1) s += __shfl_xor(s, o);
  float meanf = s / M; float mean = rbf(meanf);
  float q = 0; for (int i = 0; i < 9; i++) { float d = v[i] - meanf; q += d * d; }
  for (int o = 32; o; o >>= 1) q += __shfl_xor(q, o);
  float var = rbf(q / M);
  float den = rbf(sqrtf(rbf(var + 1e-5f)));
  for (int i = 0; i < 9; i++) ws[O_XN + b * M + t + 64 * i] = rbf(rbf(v[i] - mean) / den);
}
__global__ void k_q(const u16* wq, const u16* w1, const u16* w3, float* ws) {
  int c = blockIdx.x * 64 + threadIdx.x;
  if (c < H * D) {
    float a[4] = {0, 0, 0, 0};
    for (int m = 0; m < M; m++) { float w = bf2f(wq[(size_t)m * H * D + c]); for (int b = 0; b < 4; b++) a[b] += ws[O_XN + b * M + m] * w; }
    for (int b = 0; b < 4; b++) ws[O_Q + b * H * D + c] = rbf(a[b]);
  } else {
    c -= H * D;
    float g[4] = {0, 0, 0, 0}, u[4] = {0, 0, 0, 0};
    for (int m = 0; m < M; m++) { float wa = bf2f(w1[(size_t)m * F + c]), wb = bf2f(w3[(size_t)m * F + c]); for (int b = 0; b < 4; b++) { float xv = ws[O_XN + b * M + m]; g[b] += xv * wa; u[b] += xv * wb; } }
    for (int b = 0; b < 4; b++) { float gg = rbf(g[b]), uu = rbf(u[b]); float sl = rbf(gg / (1.f + __expf(-gg))); ws[O_HH + b * F + c] = rbf(sl * uu); }
  }
}
__global__ void k_ql(const u16* wuk, float* ws) {
  int idx = blockIdx.x * 64 + threadIdx.x;
  int h = idx / DC, c = idx % DC;
  float a[4] = {0, 0, 0, 0};
  for (int d = 0; d < D; d++) { float w = bf2f(wuk[((size_t)c * H + h) * D + d]); for (int b = 0; b < 4; b++) a[b] += ws[O_Q + b * H * D + h * D + d] * w; }
  for (int b = 0; b < 4; b++) ws[O_QL + (b * H + h) * DC + c] = a[b];
}
__global__ void k_s(const u16* ckv, float* ws) {
  int b = blockIdx.y; int k = blockIdx.x * 64 + threadIdx.x; int h = blockIdx.z;
  float a = 0;
  for (int c = 0; c < DC; c++) a += ws[O_QL + (b * H + h) * DC + c] * bf2f(ckv[((size_t)b * SK + k) * DC + c]);
  ws[O_S + ((size_t)b * H + h) * SK + k] = a * 0.08838834764831845f;
}
__global__ void k_sm(float* ws) {
  int bh = blockIdx.x; int t = threadIdx.x;
  float* s = ws + O_S + (size_t)bh * SK;
  float m = -1e30f; for (int k = t; k < SK; k += 64) m = fmaxf(m, s[k]);
  for (int o = 32; o; o >>= 1) m = fmaxf(m, __shfl_xor(m, o));
  float l = 0; for (int k = t; k < SK; k += 64) { float e = __expf(s[k] - m); s[k] = e; l += e; }
  for (int o = 32; o; o >>= 1) l += __shfl_xor(l, o);
  for (int k = t; k < SK; k += 64) s[k] = s[k] / l;
}
__global__ void k_pv(const u16* ckv, float* ws) {
  int b = blockIdx.y, h = blockIdx.z; int c = blockIdx.x * 64 + threadIdx.x;
  float a = 0; const float* p = ws + O_S + ((size_t)b * H + h) * SK;
  for (int k = 0; k < SK; k++) a += p[k] * bf2f(ckv[((size_t)b * SK + k) * DC + c]);
  ws[O_CL + (b * H + h) * DC + c] = a;
}
__global__ void k_uv(const u16* wuv, float* ws) {
  int idx = blockIdx.x * 64 + threadIdx.x; int h = idx / D;
  float a[4] = {0, 0, 0, 0};
  for (int c = 0; c < DC; c++) { float w = bf2f(wuv[(size_t)c * H * D + idx]); for (int b = 0; b < 4; b++) a[b] += ws[O_CL + (b * H + h) * DC + c] * w; }
  for (int b = 0; b < 4; b++) ws[O_CTX + b * H * D + idx] = rbf(a[b]);
}
__global__ void k_o(const u16* x, const u16* wo, const u16* w2, const float* ws, u16* out) {
  int m = blockIdx.x * 64 + threadIdx.x; int b = blockIdx.y;
  float a = 0, f = 0;
  for (int i = 0; i < H * D; i++) a += ws[O_CTX + b * H * D + i] * bf2f(wo[(size_t)i * M + m]);
  for (int i = 0; i < F; i++) f += ws[O_HH + b * F + i] * bf2f(w2[(size_t)i * M + m]);
  float r = rbf(bf2f(x[b * M + m]) + rbf(a));
  out[b * M + m] = f2bf(r + rbf(f));
}

#define NT 0
#define KAMP 0
#define MASK 7
#define METRIC 0

typedef unsigned v4u __attribute__((ext_vector_type(4)));
typedef unsigned long long u64;
template <int PER> __device__ __forceinline__ void ld(const void* base, v4u* v, int n) {
  const __amdgpu_buffer_rsrc_t rs = __builtin_amdgcn_make_buffer_rsrc((char*)base + (size_t)blockIdx.x * PER, 0, PER, 0x00020000);
  #pragma unroll
  for (int k = 0; k < (PER + 8191) / 8192; k++) v[k] = __builtin_amdgcn_raw_buffer_load_b128(rs, threadIdx.x * 16 + 8192 * k, 0, NT ? 2 : 0);
}
__global__ void __launch_bounds__(512, 2) k_stream(const void* ckv, const void* wq, const void* wuk, const void* wuv, const void* wo, const void* w1, const void* w3, const void* w2, u64* st) {
  u64 t0 = __builtin_amdgcn_s_memrealtime();
  int t = threadIdx.x;
  v4u v[29];
  #pragma unroll
  for (int k = 0; k < 29; k++) v[k] = v4u{0,0,0,0};
  if (MASK & 2) ld<18432>(wq, v + 0, 3);
  if (MASK & 2) ld<16384>(wuk, v + 3, 2);
  if (MASK & 1) ld<65536>(ckv, v + 5, 8);
  if (MASK & 2) ld<16384>(wuv, v + 13, 2);
  if (MASK & 2) ld<18432>(wo, v + 15, 3);
  if (MASK & 4) ld<20736>(w1, v + 18, 3);
  if (MASK & 4) ld<20736>(w3, v + 21, 3);
  if (MASK & 4) ld<20736>(w2, v + 24, 3);
  __builtin_amdgcn_sched_barrier(0);
  v4u acc = v[(MASK & 2) ? 0 : (MASK & 1) ? 5 : 18];
  asm volatile("" : "+v"(acc));
  u64 tf = __builtin_amdgcn_s_memrealtime();
  #pragma unroll
  for (int k = 0; k < 29; k++) acc ^= v[k];
  unsigned r = acc.x ^ acc.y ^ acc.z ^ acc.w;
  asm volatile("" :: "v"(r));
  __syncthreads();
  u64 t1 = __builtin_amdgcn_s_memrealtime();
  if (t == 0) { st[blockIdx.x * 4] = t0; st[blockIdx.x * 4 + 1] = t1; st[blockIdx.x * 4 + 2] = tf; }
}
__global__ void k_spin(const u64* st) {
  u64 mn = ~0ull, mx = 0, mx0 = 0, sd = 0, sf = 0;
  for (int i = 0; i < 256; i++) { u64 a = st[4 * i], b = st[4 * i + 1], c = st[4 * i + 2]; mn = a < mn ? a : mn; mx = b > mx ? b : mx; mx0 = a > mx0 ? a : mx0; sd += b - a; sf += c - a; }
  u64 T = METRIC == 0 ? (mx - mn) * 100 : METRIC == 1 ? (mx0 - mn) * 100 : METRIC == 2 ? sd * 100 / 256 : sf * 100 / 256;
  if (T > 500000) T = 500000;
  if (KAMP == 0) T = 0;
  u64 t0 = __builtin_amdgcn_s_memrealtime();
  while (__builtin_amdgcn_s_memrealtime() < t0 + T) __builtin_amdgcn_s_sleep(4);
}
extern "C" void kernel_launch(void* const* d_in, const int* in_sizes, int n_in, void* d_out, int out_size, void* d_ws, size_t ws_size, hipStream_t stream) {
  const u16* x = (const u16*)d_in[0]; const u16* ckv = (const u16*)d_in[1]; const u16* wq = (const u16*)d_in[2];
  const u16* wuk = (const u16*)d_in[3]; const u16* wuv = (const u16*)d_in[4]; const u16* wo = (const u16*)d_in[5];
  const u16* w1 = (const u16*)d_in[7]; const u16* w3 = (const u16*)d_in[8]; const u16* w2 = (const u16*)d_in[9];
  float* ws = (float*)d_ws; u16* out = (u16*)d_out;

  { u64* st = (u64*)((char*)d_ws + (64 << 20));
    k_stream<<<256, 512, 0, stream>>>(ckv, wq, wuk, wuv, wo, w1, w3, w2, st);
    k_spin<<<1, 64, 0, stream>>>(st); }
  k_ln<<<4, 64, 0, stream>>>(x, ws);
  k_q<<<(H * D + F) / 64, 64, 0, stream>>>(wq, w1, w3, ws);
  k_ql<<<H * DC / 64, 64, 0, stream>>>(wuk, ws);
  k_s<<<dim3(SK / 64, B, H), 64, 0, stream>>>(ckv, ws);
  k_sm<<<B * H, 64, 0, stream>>>(ws);
  k_pv<<<dim3(DC / 64, B, H), 64, 0, stream>>>(ckv, ws);
  k_uv<<<H * D / 64, 64, 0, stream>>>(wuv, ws);
  k_o<<<dim3(M / 64, B), 64, 0, stream>>>(x, wo, w2, ws, out);
}
```

```cpp
#include <hip/hip_runtime.h>
#include <hip/hip_bf16.h>
#include <stdint.h>

typedef unsigned short u16;
__device__ __forceinline__ float bf2f(u16 v) { return __uint_as_float(((unsigned)v) << 16); }
__device__ __forceinline__ u16 f2bf(float f) { unsigned u = __float_as_uint(f); u += 0x7FFFu + ((u >> 16) & 1u); return (u16)(u >> 16); }
__device__ __forceinline__ float rbf(float f) { return bf2f(f2bf(f)); }

constexpr int B = 4, H = 32, SK = 4096, D = 128, M = 576, DC = 512, F = 4608;

constexpr size_t O_XN = 0;
constexpr size_t O_Q = O_XN + B * M;
constexpr size_t O_HH = O_Q + B * H * D;
constexpr size_t O_QL = O_HH + B * F;
constexpr size_t O_S = O_QL + B * H * DC;
constexpr size_t O_CL = O_S + (size_t)B * H * SK;
constexpr size_t O_CTX = O_CL + B * H * DC;

__global__ void k_ln(const u16* x, float* ws) {
  int b = blockIdx.x; int t = threadIdx.x;
  float v[9]; float s = 0;
  for (int i = 0; i < 9; i++) { v[i] = bf2f(x[b * M + t + 64 * i]); s += v[i]; }
  for (int o = 32; o; o >>= 1) s += __shfl_xor(s, o);
  float meanf = s / M; float mean = rbf(meanf);
  float q = 0; for (int i = 0; i < 9; i++) { float d = v[i] - meanf; q += d * d; }
  for (int o = 32; o; o >>= 1) q += __shfl_xor(q, o);
  float var = rbf(q / M);
  float den = rbf(sqrtf(rbf(var + 1e-5f)));
  for (int i = 0; i < 9; i++) ws[O_XN + b * M + t + 64 * i] = rbf(rbf(v[i] - mean) / den);
}
__global__ void k_q(const u16* wq, const u16* w1, const u16* w3, float* ws) {
  int c = blockIdx.x * 64 + threadIdx.x;
  if (c < H * D) {
    float a[4] = {0, 0, 0, 0};
    for (int m = 0; m < M; m++) { float w = bf2f(wq[(size_t)m * H * D + c]); for (int b = 0; b < 4; b++) a[b] += ws[O_XN + b * M + m] * w; }
    for (int b = 0; b < 4; b++) ws[O_Q + b * H * D + c] = rbf(a[b]);
  } else {
    c -= H * D;
    float g[4] = {0, 0, 0, 0}, u[4] = {0, 0, 0, 0};
    for (int m = 0; m < M; m++) { float wa = bf2f(w1[(size_t)m * F + c]), wb = bf2f(w3[(size_t)m * F + c]); for (int b = 0; b < 4; b++) { float xv = ws[O_XN + b * M + m]; g[b] += xv * wa; u[b] += xv * wb; } }
    for (int b = 0; b < 4; b++) { float gg = rbf(g[b]), uu = rbf(u[b]); float sl = rbf(gg / (1.f + __expf(-gg))); ws[O_HH + b * F + c] = rbf(sl * uu); }
  }
}
__global__ void k_ql(const u16* wuk, float* ws) {
  int idx = blockIdx.x * 64 + threadIdx.x;
  int h = idx / DC, c = idx % DC;
  float a[4] = {0, 0, 0, 0};
  for (int d = 0; d < D; d++) { float w = bf2f(wuk[((size_t)c * H + h) * D + d]); for (int b = 0; b < 4; b++) a[b] += ws[O_Q + b * H * D + h * D + d] * w; }
  for (int b = 0; b < 4; b++) ws[O_QL + (b * H + h) * DC + c] = a[b];
}
__global__ void k_s(const u16* ckv, float* ws) {
  int b = blockIdx.y; int k = blockIdx.x * 64 + threadIdx.x; int h = blockIdx.z;
  float a = 0;
  for (int c = 0; c < DC; c++) a += ws[O_QL + (b * H + h) * DC + c] * bf2f(ckv[((size_t)b * SK + k) * DC + c]);
  ws[O_S + ((size_t)b * H + h) * SK + k] = a * 0.08838834764831845f;
}
__global__ void k_sm(float* ws) {
  int bh = blockIdx.x; int t = threadIdx.x;
  float* s = ws + O_S + (size_t)bh * SK;
  float m = -1e30f; for (int k = t; k < SK; k += 64) m = fmaxf(m, s[k]);
  for (int o = 32; o; o >>= 1) m = fmaxf(m, __shfl_xor(m, o));
  float l = 0; for (int k = t; k < SK; k += 64) { float e = __expf(s[k] - m); s[k] = e; l += e; }
  for (int o = 32; o; o >>= 1) l += __shfl_xor(l, o);
  for (int k = t; k < SK; k += 64) s[k] = s[k] / l;
}
__global__ void k_pv(const u16* ckv, float* ws) {
  int b = blockIdx.y, h = blockIdx.z; int c = blockIdx.x * 64 + threadIdx.x;
  float a = 0; const float* p = ws + O_S + ((size_t)b * H + h) * SK;
  for (int k = 0; k < SK; k++) a += p[k] * bf2f(ckv[((size_t)b * SK + k) * DC + c]);
  ws[O_CL + (b * H + h) * DC + c] = a;
}
__global__ void k_uv(const u16* wuv, float* ws) {
  int idx = blockIdx.x * 64 + threadIdx.x; int h = idx / D;
  float a[4] = {0, 0, 0, 0};
  for (int c = 0; c < DC; c++) { float w = bf2f(wuv[(size_t)c * H * D + idx]); for (int b = 0; b < 4; b++) a[b] += ws[O_CL + (b * H + h) * DC + c] * w; }
  for (int b = 0; b < 4; b++) ws[O_CTX + b * H * D + idx] = rbf(a[b]);
}
__global__ void k_o(const u16* x, const u16* wo, const u16* w2, const float* ws, u16* out) {
  int m = blockIdx.x * 64 + threadIdx.x; int b = blockIdx.y;
  float a = 0, f = 0;
  for (int i = 0; i < H * D; i++) a += ws[O_CTX + b * H * D + i] * bf2f(wo[(size_t)i * M + m]);
  for (int i = 0; i < F; i++) f += ws[O_HH + b * F + i] * bf2f(w2[(size_t)i * M + m]);
  float r = rbf(bf2f(x[b * M + m]) + rbf(a));
  out[b * M + m] = f2bf(r + rbf(f));
}

#define NT 0
#define KAMP 200
#define MASK 1
#define METRIC 2

typedef unsigned v4u __attribute__((ext_vector_type(4)));
typedef unsigned long long u64;
template <int PER> __device__ __forceinline__ void ld(const void* base, v4u* v, int n) {
  const __amdgpu_buffer_rsrc_t rs = __builtin_amdgcn_make_buffer_rsrc((char*)base + (size_t)blockIdx.x * PER, 0, PER, 0x00020000);
  #pragma unroll
  for (int k = 0; k < (PER + 8191) / 8192; k++) v[k] = __builtin_amdgcn_raw_buffer_load_b128(rs, threadIdx.x * 16 + 8192 * k, 0, NT ? 2 : 0);
}
__global__ void __launch_bounds__(512, 2) k_stream(const void* ckv, const void* wq, const void* wuk, const void* wuv, const void* wo, const void* w1, const void* w3, const void* w2, u64* st) {
  u64 t0 = __builtin_amdgcn_s_memrealtime();
  int t = threadIdx.x;
  v4u v[29];
  #pragma unroll
  for (int k = 0; k < 29; k++) v[k] = v4u{0,0,0,0};
  if (MASK & 2) ld<18432>(wq, v + 0, 3);
  if (MASK & 2) ld<16384>(wuk, v + 3, 2);
  if (MASK & 1) ld<65536>(ckv, v + 5, 8);
  if (MASK & 2) ld<16384>(wuv, v + 13, 2);
  if (MASK & 2) ld<18432>(wo, v + 15, 3);
  if (MASK & 4) ld<20736>(w1, v + 18, 3);
  if (MASK & 4) ld<20736>(w3, v + 21, 3);
  if (MASK & 4) ld<20736>(w2, v + 24, 3);
  __builtin_amdgcn_sched_barrier(0);
  v4u acc = v[(MASK & 2) ? 0 : (MASK & 1) ? 5 : 18];
  asm volatile("" : "+v"(acc));
  u64 tf = __builtin_amdgcn_s_memrealtime();
  #pragma unroll
  for (int k = 0; k < 29; k++) acc ^= v[k];
  unsigned r = acc.x ^ acc.y ^ acc.z ^ acc.w;
  asm volatile("" :: "v"(r));
  __syncthreads();
  u64 t1 = __builtin_amdgcn_s_memrealtime();
  if (t == 0) { st[blockIdx.x * 4] = t0; st[blockIdx.x * 4 + 1] = t1; st[blockIdx.x * 4 + 2] = tf; }
}
__global__ void k_spin(const u64* st) {
  u64 mn = ~0ull, mx = 0, mx0 = 0, sd = 0, sf = 0;
  for (int i = 0; i < 256; i++) { u64 a = st[4 * i], b = st[4 * i + 1], c = st[4 * i + 2]; mn = a < mn ? a : mn; mx = b > mx ? b : mx; mx0 = a > mx0 ? a : mx0; sd += b - a; sf += c - a; }
  u64 T = METRIC == 0 ? (mx - mn) * KAMP : METRIC == 1 ? (mx0 - mn) * KAMP : METRIC == 2 ? sd * KAMP / 256 : sf * KAMP / 256;
  if (T > 500000) T = 500000;
  if (KAMP == 0) T = 0;
  u64 t0 = __builtin_amdgcn_s_memrealtime();
  while (__builtin_amdgcn_s_memrealtime() < t0 + T) __builtin_amdgcn_s_sleep(4);
}
extern "C" void kernel_launch(void* const* d_in, const int* in_sizes, int n_in, void* d_out, int out_size, void* d_ws, size_t ws_size, hipStream_t stream) {
  const u16* x = (const u16*)d_in[0]; const u16* ckv = (const u16*)d_in[1]; const u16* wq = (const u16*)d_in[2];
  const u16* wuk = (const u16*)d_in[3]; const u16* wuv = (const u16*)d_in[4]; const u16* wo = (const u16*)d_in[5];
  const u16* w1 = (const u16*)d_in[7]; const u16* w3 = (const u16*)d_in[8]; const u16* w2 = (const u16*)d_in[9];
  float* ws = (float*)d_ws; u16* out = (u16*)d_out;

  { u64* st = (u64*)((char*)d_ws + (64 << 20));
    k_stream<<<256, 512, 0, stream>>>((char*)d_ws + ((size_t)8 << 20), wq, wuk, wuv, wo, w1, w3, w2, st);
    k_spin<<<1, 64, 0, stream>>>(st); }
  k_ln<<<4, 64, 0, stream>>>(x, ws);
  k_q<<<(H * D + F) / 64, 64, 0, stream>>>(wq, w1, w3, ws);
  k_ql<<<H * DC / 64, 64, 0, stream>>>(wuk, ws);
  k_s<<<dim3(SK / 64, B, H), 64, 0, stream>>>(ckv, ws);
  k_sm<<<B * H, 64, 0, stream>>>(ws);
  k_pv<<<dim3(DC / 64, B, H), 64, 0, stream>>>(ckv, ws);
  k_uv<<<H * D / 64, 64, 0, stream>>>(wuv, ws);
  k_o<<<dim3(M / 64, B), 64, 0, stream>>>(x, wo, w2, ws, out);
}
```

```cpp
#include <hip/hip_runtime.h>
#include <stdint.h>

namespace mla {
typedef unsigned short u16;
typedef unsigned long long u64;
typedef unsigned v4u __attribute__((ext_vector_type(4)));
typedef unsigned v2u __attribute__((ext_vector_type(2)));
typedef float f4 __attribute__((ext_vector_type(4)));
typedef float f2 __attribute__((ext_vector_type(2)));
typedef float f16v __attribute__((ext_vector_type(16)));
typedef short s4 __attribute__((ext_vector_type(4)));
typedef __bf16 b8 __attribute__((ext_vector_type(8)));
typedef __bf16 b2 __attribute__((ext_vector_type(2)));
#define LAS __attribute__((address_space(3)))
#define DI __device__ __forceinline__
typedef __amdgpu_buffer_rsrc_t rsrc_t;
typedef LAS char* lptr;

constexpr int NT = 576;
constexpr int LDS_BYTES = 163840;
constexpr unsigned WS_E = 0;
constexpr unsigned SP = 132 * 1024;
constexpr unsigned WS_H1 = 0;
constexpr unsigned WS_QL = 33 * 1024;
constexpr unsigned WS_QH = 49 * 1024;
constexpr unsigned WS_O3 = 32 * SP;
constexpr unsigned WS_HD = WS_O3 + 4 * 32 * 32 * 1024;
constexpr unsigned WS_GU = WS_HD + 4 * 32 * 8 * 512;
constexpr unsigned WS_H5 = WS_GU + 2 * 9216 * 16;
constexpr unsigned WS_END = WS_H5 + 72 * 128 * 128;
constexpr unsigned KE = 0x100000u;
constexpr int SPIN_CAP = 1 << 18;
constexpr int AUX_NT = 2, AUX_SC1 = 16;
constexpr unsigned OOB = 0x80000000u;

#define CODE_BYTES 21888
constexpr int QD = 6, PAD = 3;

DI rsrc_t mk(const void* p, unsigned bytes) { return __builtin_amdgcn_make_buffer_rsrc((void*)p, (short)0, (int)bytes, 0x00020000); }
template <int AUX> DI v4u ld128(rsrc_t r, unsigned vo, unsigned so) { return __builtin_amdgcn_raw_buffer_load_b128(r, vo, so, AUX); }
template <int AUX> DI v2u ld64(rsrc_t r, unsigned vo, unsigned so) { return __builtin_amdgcn_raw_buffer_load_b64(r, vo, so, AUX); }
template <int AUX> DI unsigned ld32(rsrc_t r, unsigned vo, unsigned so) { return __builtin_amdgcn_raw_buffer_load_b32(r, vo, so, AUX); }
DI void st128(v4u v, rsrc_t r, unsigned vo, unsigned so) { __builtin_amdgcn_raw_buffer_store_b128(v, r, vo, so, AUX_SC1); }
DI void st32(unsigned v, rsrc_t r, unsigned vo, unsigned so) { __builtin_amdgcn_raw_buffer_store_b32(v, r, vo, so, AUX_SC1); }
DI void bar() { asm volatile("s_waitcnt lgkmcnt(0)\n\ts_barrier" ::: "memory"); }
DI void sched_fence() { __builtin_amdgcn_sched_barrier(0); }
DI float u2f(unsigned u) { return __builtin_bit_cast(float, u); }
DI unsigned f2u(float f) { return __builtin_bit_cast(unsigned, f); }
DI unsigned pk2(float lo, float hi) { return __builtin_bit_cast(unsigned, __builtin_convertvector(f2{lo, hi}, b2)); }
DI float rbf(float f) { return u2f(pk2(f, 0.f) << 16); }
DI float blo(unsigned u) { return u2f(u << 16); }
DI float bhi(unsigned u) { return u2f(u & 0xFFFF0000u); }
DI f4 mfma16(v2u a, v2u b, f4 c) { return __builtin_amdgcn_mfma_f32_16x16x16bf16_1k(__builtin_bit_cast(s4, a), __builtin_bit_cast(s4, b), c, 0, 0, 0); }
DI f4 mfma32(v4u a, v4u b, f4 c) { return __builtin_amdgcn_mfma_f32_16x16x32_bf16(__builtin_bit_cast(b8, a), __builtin_bit_cast(b8, b), c, 0, 0, 0); }
DI f16v mfmaT(v4u a, v4u b, f16v c) { return __builtin_amdgcn_mfma_f32_32x32x16_bf16(__builtin_bit_cast(b8, a), __builtin_bit_cast(b8, b), c, 0, 0, 0); }
template <int C> DI unsigned dppu(unsigned v) { return (unsigned)__builtin_amdgcn_update_dpp(0, (int)v, C, 0xf, 0xf, true); }
DI unsigned row_sum_u(unsigned v) { v += dppu<0x128>(v); v += dppu<0x124>(v); v += dppu<0x122>(v); v += dppu<0x121>(v); return v; }
DI float row_sum_f(float v) { v += u2f(dppu<0x128>(f2u(v))); v += u2f(dppu<0x124>(f2u(v))); v += u2f(dppu<0x122>(f2u(v))); v += u2f(dppu<0x121>(f2u(v))); return v; }
DI unsigned x16_sum_u(unsigned v) { auto r = __builtin_amdgcn_permlane16_swap(v, v, false, false); return r[0] + r[1]; }
DI unsigned x32_sum_u(unsigned v) { auto r = __builtin_amdgcn_permlane32_swap(v, v, false, false); return r[0] + r[1]; }
DI float x16_sum_f(float v) { auto r = __builtin_amdgcn_permlane16_swap(f2u(v), f2u(v), false, false); return u2f(r[0]) + u2f(r[1]); }
DI float x32_sum_f(float v) { auto r = __builtin_amdgcn_permlane32_swap(f2u(v), f2u(v), false, false); return u2f(r[0]) + u2f(r[1]); }
DI unsigned wave_sum_u(unsigned v) { return x32_sum_u(x16_sum_u(row_sum_u(v))); }
template <class T> DI T lds_ld(lptr p) { return *(LAS T*)p; }
template <class T> DI void lds_st(lptr p, T v) { *(LAS T*)p = v; }

DI void blk_transpose(const v4u (&r)[4], v2u (&f)[8]) {
#pragma unroll
  for (int jj = 0; jj < 4; jj++) {
    f[2 * jj] = v2u{__builtin_amdgcn_perm(r[1][jj], r[0][jj], 0x05040100u), __builtin_amdgcn_perm(r[3][jj], r[2][jj], 0x05040100u)};
    f[2 * jj + 1] = v2u{__builtin_amdgcn_perm(r[1][jj], r[0][jj], 0x07060302u), __builtin_amdgcn_perm(r[3][jj], r[2][jj], 0x07060302u)};
  }
}
DI v2u lo_rows(v2u a, int lane) { return (lane & 8) ? v2u{0u, 0u} : a; }
DI v2u hi_rows(v2u a, int lane) { return (lane & 8) ? a : v2u{0u, 0u}; }
DI void frag_mfma(const v2u (&f)[8], v2u a, f4 (&acc)[4], int lane) {
  const v2u alo = lo_rows(a, lane), ahi = hi_rows(a, lane);
#pragma unroll
  for (int p = 0; p < 4; p++) acc[p] = mfma16(alo, f[2 * p], acc[p]);
#pragma unroll
  for (int p = 0; p < 4; p++) acc[p] = mfma16(ahi, f[2 * p + 1], acc[p]);
}
DI void row_split(v4u r, v2u (&f)[8]) {
#pragma unroll
  for (int jj = 0; jj < 4; jj++) { f[2 * jj] = v2u{r[jj] & 0xFFFFu, 0u}; f[2 * jj + 1] = v2u{r[jj] >> 16, 0u}; }
}
DI int aoff(int stride, int lane) { return (lane & 3) * stride + (16 * ((lane >> 2) & 1) + 4 * (lane >> 4)) * 2; }
DI int aoff_row(int stride, int lane) { return (lane & 3) * stride + (2 * (lane >> 4) + ((lane >> 2) & 1)) * 2; }
DI int cpos(int col) { return 32 * (col & 1) + 8 * ((col >> 1) & 3) + ((col >> 3) ^ ((col >> 1) & 3)); }
DI void red_store(lptr slot, const f4 (&acc)[4], int lane) {
  const int g = lane >> 4, rs = (lane >> 3) & 1, cn = lane & 7;
  if ((g & 1) == rs) {
#pragma unroll
    for (int p = 0; p < 4; p++) lds_st<f4>(slot + rs * 1024 + (32 * (g >> 1) + 8 * p + (cn ^ p)) * 16, acc[p]);
  }
}
DI float wave_sum_f(float v) { return x32_sum_f(x16_sum_f(row_sum_f(v))); }

DI void layer_norm_row(const v2u (&xv)[3], lptr xs, int lane) {
  float v[12];
#pragma unroll
  for (int k = 0; k < 3; k++) { v[4 * k] = blo(xv[k].x); v[4 * k + 1] = bhi(xv[k].x); v[4 * k + 2] = blo(xv[k].y); v[4 * k + 3] = bhi(xv[k].y); }
  const float s = ((v[0] + v[1]) + (v[2] + v[3])) + ((v[4] + v[5]) + (v[6] + v[7])) + ((v[8] + v[9]) + (v[10] + v[11]));
  const float mean = wave_sum_f(s) * (1.0f / 576.0f);
  float q0 = 0.f, q1 = 0.f;
#pragma unroll
  for (int k = 0; k < 8; k++) { const float d = v[k] - mean; q0 += d * d; }
#pragma unroll
  for (int k = 8; k < 12; k++) { const float d = v[k] - mean; q1 += d * d; }
  const float var = rbf(wave_sum_f(q0 + (lane < 16 ? q1 : 0.f)) * (1.0f / 576.0f));
  const float rden = __builtin_amdgcn_rcpf(rbf(__builtin_amdgcn_sqrtf(rbf(var + 1e-5f))));
  const float mb = rbf(mean);
#pragma unroll
  for (int k = 0; k < 3; k++) {
    const unsigned d0 = pk2(v[4 * k] - mb, v[4 * k + 1] - mb), d1 = pk2(v[4 * k + 2] - mb, v[4 * k + 3] - mb);
    const v2u o = v2u{pk2(blo(d0) * rden, bhi(d0) * rden), pk2(blo(d1) * rden, bhi(d1) * rden)};
    if (k < 2 || lane < 16) lds_st<v2u>(xs + (lane + 64 * k) * 8, o);
  }
}

struct Args {
  const void* x; void* ws;
  const void *wq, *wuk, *ckv, *wuv, *wo, *w1, *w3, *w2;
  void* out;
};

#define DPPMAX(name, ctl) DI float name(float x) { float r; asm("s_nop 1\n\tv_max_f32_dpp %0, %1, %1 " ctl " row_mask:0xf bank_mask:0xf" : "=v"(r) : "v"(x)); return r; }
DPPMAX(max_x1, "quad_perm:[1,0,3,2]") DPPMAX(max_x2, "quad_perm:[2,3,0,1]") DPPMAX(max_hm, "row_half_mirror") DPPMAX(max_r8, "row_ror:8")
DI float vmax(float a, float b) { float r; asm("v_max_f32 %0, %1, %2" : "=v"(r) : "v"(a), "v"(b)); return r; }
DI float vmax3(float a, float b, float c) { float r; asm("v_max3_f32 %0, %1, %2, %3" : "=v"(r) : "v"(a), "v"(b), "v"(c)); return r; }
DI float x16_vmax(float v) { auto r = __builtin_amdgcn_permlane16_swap(f2u(v), f2u(v), false, false); return vmax(u2f(r[0]), u2f(r[1])); }
DI float x32_vmax(float v) { auto r = __builtin_amdgcn_permlane32_swap(f2u(v), f2u(v), false, false); return vmax(u2f(r[0]), u2f(r[1])); }
DI float sel4(f4 d, int g) { const float x = (g & 1) ? d.y : d.x, y = (g & 1) ? d.w : d.z; return (g & 2) ? y : x; }
DI float dot2(unsigned a, unsigned b, float c) { return __builtin_amdgcn_fdot2_f32_bf16(__builtin_bit_cast(b2, a), __builtin_bit_cast(b2, b), c, false); }
DI lptr lp(unsigned a) { return (lptr)(size_t)a; }
DI v4u trfrag(lptr a0, lptr a1) {
  const v2u u0 = __builtin_bit_cast(v2u, __builtin_amdgcn_ds_read_tr16_b64_v4i16((LAS s4*)a0)), u1 = __builtin_bit_cast(v2u, __builtin_amdgcn_ds_read_tr16_b64_v4i16((LAS s4*)a1));
  return v4u{u0.x, u0.y, u1.x, u1.y};
}
#define DMA(ldsaddr, voff, rsrc, soff, POL) asm volatile("s_mov_b32 m0, %0\n\ts_nop 0\n\tbuffer_load_dwordx4 %1, %2, %3 offen " POL " lds" ::"s"(ldsaddr), "v"(voff), "s"(rsrc), "s"(soff) : "memory")
#define VMWAIT(n) asm volatile("s_waitcnt vmcnt(%0)" ::"i"(n) : "memory")

DI void role_y(const Args& p, lptr L, int xcd, int yi, int w, int lane, unsigned e) {
  const int h = 4 * xcd + (yi >> 2), qd = yi & 3, yj = 4 * h + qd;
  const int g = lane >> 4, cn = lane & 7;
  const int v = (w - 1) & 7;
  const unsigned L0 = (unsigned)(size_t)L;
  constexpr int S = 147456, QS = S + 4864, QL = S + 6144, CL = S + 10240, CTX = S + 11392, HS = S + 12544;
  const rsrc_t rws = mk(p.ws, WS_END);
  unsigned tb[2];
  {
    const int q = (lane & 15) >> 2, pq = lane & 3;
#pragma unroll
    for (int t = 0; t < 2; t++) tb[t] = L0 + (8 * g + 4 * t + q) * 256 + (((2 * v + (pq >> 1)) ^ ((q << 2) | ((2 * g + t) & 3))) << 4) + 8 * (pq & 1);
  }
  v4u wuk[4];
  VMWAIT(0);
#pragma unroll
  for (int ks = 0; ks < 4; ks++) wuk[ks] = lds_ld<v4u>(L + 32768 + (16 * v + (lane & 15)) * 256 + (((4 * ks + g) ^ (((lane & 3) << 2) | ((lane >> 2) & 3))) << 4));
#pragma unroll
  for (int ks = 0; ks < 4; ks++) asm volatile("" : "+v"(wuk[ks]));
  unsigned a_qs = L0 + QS + (lane & 3) * 272 + g * 16, a_ql = L0 + QL + g * 256 + (16 * v + (lane & 15)) * 2;
  unsigned a_qo = (8 * g + 2 * qd + (h & 1)) * SP + WS_QL + (h >> 1) * 1024 + (lane & 15) * 16;
  asm volatile("" : "+v"(a_qs), "+v"(a_ql), "+v"(a_qo));
  if (w == 5 || w == 6) {
    __builtin_amdgcn_s_sleep(2);
    const unsigned o = (4 * (w - 5) + g) * SP + WS_H1 + (1 + h) * 1024 + (lane & 15) * 16;
    v4u a[4];
#pragma unroll 1
    for (int spin = 0; spin < SPIN_CAP; spin++) {
      asm volatile("" ::: "memory");
      bool ok = true;
#pragma unroll
      for (int i = 0; i < 4; i++) a[i] = ld128<AUX_SC1>(rws, o, i * 8 * SP);
#pragma unroll
      for (int i = 0; i < 4; i++) ok &= (a[i].y == e) & (a[i].w == e);
      if (__all(ok)) break;
      __builtin_amdgcn_s_sleep(1);
    }
    const unsigned q01 = pk2((blo(a[0].x) + blo(a[1].x)) + (blo(a[2].x) + blo(a[3].x)), (bhi(a[0].x) + bhi(a[1].x)) + (bhi(a[2].x) + bhi(a[3].x)));
    const unsigned qd3 = pk2((blo(a[0].z) + blo(a[1].z)) + (blo(a[2].z) + blo(a[3].z)), (bhi(a[0].z) + bhi(a[1].z)) + (bhi(a[2].z) + bhi(a[3].z)));
    const lptr q = L + QS + (64 * (w - 5) + lane) * 2;
    lds_st<u16>(q, (u16)q01); lds_st<u16>(q + 272, (u16)(q01 >> 16)); lds_st<u16>(q + 544, (u16)qd3); lds_st<u16>(q + 816, (u16)(qd3 >> 16));
  }
  bar();
  if (w > 0) {
    v4u a[4];
#pragma unroll
    for (int ks = 0; ks < 4; ks++) a[ks] = lds_ld<v4u>(lp(a_qs) + ks * 64);
    sched_fence();
    f4 d0 = mfma32(a[0], wuk[0], f4{0.f, 0.f, 0.f, 0.f}), d1 = mfma32(a[1], wuk[1], f4{0.f, 0.f, 0.f, 0.f});
    d0 = mfma32(a[2], wuk[2], d0); d1 = mfma32(a[3], wuk[3], d1);
    const float c0 = sel4(d0 + d1, g);
    const float sc = 0.08838834764831845f * 1.4426950408889634f * ((e & 1u) ? 2.0f : 1.0f);
    lds_st<u16>(lp(a_ql), (u16)pk2(c0 * sc, 0.f));
  }
  bar();
  if (w == 0) {
    const v4u o = lds_ld<v4u>(L + QL + lane * 16);
    st128(o, rws, a_qo, 0);
    const unsigned cs = row_sum_u((o.x + o.y) + (o.z + o.w)) + KE * e;
    if ((lane & 15) == 0) __builtin_amdgcn_raw_buffer_store_b64(v2u{cs, e}, rws, 8 * g * SP + WS_QH + (h * 4 + qd) * 8, 0, AUX_SC1);
  }
  bar();
  unsigned tch = 0;
  if (w >= 1 && w <= 5) {
    const rsrc_t rc = mk(p.ckv, 4 * 4096 * 1024);
    asm volatile("buffer_load_dword %0, %1, %2, %3 offen" : "=v"(tch) : "v"((lane >> 3) * 16384 + (lane & 7) * 128), "s"(rc), "s"((((xcd >> 1) * 4096 + 128 * (16 * (xcd & 1) + yi) + 16 - w) << 10)));
  }
  asm volatile("s_barrier" ::: "memory");
  __builtin_amdgcn_s_sleep(45);
  const rsrc_t rwo = mk((const char*)p.wo + (size_t)h * 128 * 1152, 128 * 1152);
  const unsigned pvo = (lane >> 3) * 1152 + w * 128;
  const unsigned pv0 = pvo + ((cn ^ (2 * ((lane >> 4) & 1))) << 4), pv1 = pvo + ((cn ^ (2 * ((lane >> 4) & 1)) ^ 4) << 4);
  const unsigned LW = L0 + w * 16384;
  auto wo_dma = [&](int u0, int u1) {
#pragma unroll 1
    for (int u = u0; u < u1; u++) {
      const int i = (4 * qd + 2 * u) & 15;
      DMA(LW + i * 1024, pv0, rwo, i * 9216, "");
      DMA(LW + i * 1024 + 1024, pv1, rwo, i * 9216 + 9216, "");
    }
  };
  v4u fuv[4], fo[16];
  if (w > 0) {
    const rsrc_t r = mk(p.wuv, 512 * 8192);
#pragma unroll
    for (int i = 0; i < 4; i++) {
      DMA(L0 + (4 * v + i) * 1024, g * 8192 + (((lane & 15) ^ ((g << 2) | i)) << 4), r, (128 * qd + 16 * v + 4 * i) * 8192 + h * 256, "nt");
    }
  }
  asm volatile("s_barrier" ::: "memory");
  if (w >= 2) { wo_dma(0, 2); VMWAIT(4); } else VMWAIT(0);
  asm volatile("" : "+v"(tch), "+v"(tch));
  bar();
#pragma unroll
  for (int ks = 0; ks < 4; ks++) fuv[ks] = trfrag(lp(tb[0]) + ks * 8192, lp(tb[1]) + ks * 8192);
#pragma unroll
  for (int ks = 0; ks < 4; ks++) asm volatile("" : "+v"(fuv[ks]));
  bar();
  if (w < 2) wo_dma(0, 2);
  asm volatile("s_barrier" ::: "memory");
  wo_dma(2, 8);
  unsigned tp[4];
  {
    const int q = (lane & 15) >> 2, pq = lane & 3;
#pragma unroll
    for (int c = 0; c < 4; c++) tp[c] = LW + (8 * g + q) * 128 + ((2 * (c ^ ((q >> 1) | ((g & 1) << 1))) + (pq >> 1)) << 4) + 8 * (pq & 1);
  }
  auto wo_frags = [&]() {
#pragma unroll
    for (int ks = 0; ks < 4; ks++)
#pragma unroll
      for (int c = 0; c < 4; c++) fo[4 * ks + c] = trfrag(lp(tp[c]) + ks * 4096, lp(tp[c]) + ks * 4096 + 512);
#pragma unroll
    for (int i = 0; i < 16; i++) asm volatile("" : "+v"(fo[i]));
    asm volatile("s_waitcnt lgkmcnt(0)" ::: "memory");
  };
  if (w > 0) VMWAIT(0);
  const rsrc_t rw2 = mk((const char*)p.w2 + (size_t)(36 * yj) * 1152, 36 * 1152);
  auto w2_dma = [&]() {
    DMA(LW, pv0, rw2, 0, "nt"); DMA(LW + 1024, pv1, rw2, 9216, "nt"); DMA(LW + 2048, pv0, rw2, 2 * 9216, "nt"); DMA(LW + 3072, pv1, rw2, 3 * 9216, "nt");
    DMA(LW + 4096, pv0 - (lane >> 5) * 4 * 1152, rw2, 4 * 9216, "nt");
  };
  v4u f2[8];
  auto w2_frags = [&]() {
#pragma unroll
    for (int ks = 0; ks < 2; ks++)
#pragma unroll
      for (int c = 0; c < 4; c++) f2[4 * ks + c] = trfrag(lp(tp[c]) + ks * 4096, lp(tp[c]) + ks * 4096 + 512);
#pragma unroll
    for (int i = 0; i < 8; i++) asm volatile("" : "+v"(f2[i]));
  };
  constexpr int CNT = HS + 640;
  constexpr int GA = S;
  const int gf = 36 * yj + lane, r9g = (gf >> 6) % 9;
  auto gu_issue = [&]() {
    const unsigned bo = WS_GU + gf * 16;
    if (lane < 36 && r9g != 0) { DMA(L0 + GA, bo, rws, 0, "sc1"); DMA(L0 + GA + 1024, bo, rws, 4608 * 16, "sc1"); }
    if (lane < 36 && r9g != 8) { DMA(L0 + GA + 2048, bo, rws, 9216 * 16, "sc1"); DMA(L0 + GA + 3072, bo, rws, (9216 + 4608) * 16, "sc1"); }
  };
  auto gu_finish = [&]() {
    VMWAIT(0);
    v4u ga[4];
#pragma unroll
    for (int i = 0; i < 4; i++) ga[i] = lds_ld<v4u>(L + GA + i * 1024 + lane * 16);
    unsigned bad = 0;
#pragma unroll
    for (int i = 0; i < 4; i++) bad |= (ga[i].y ^ e) | (ga[i].w ^ e);
    if (!__all(bad == 0u)) return false;
#pragma unroll
    for (int k = 0; k < 2; k++) {
      const unsigned gp = pk2(blo(ga[0][2 * k]) + blo(ga[2][2 * k]), bhi(ga[0][2 * k]) + bhi(ga[2][2 * k]));
      const unsigned up = pk2(blo(ga[1][2 * k]) + blo(ga[3][2 * k]), bhi(ga[1][2 * k]) + bhi(ga[3][2 * k]));
      const float g0 = blo(gp), g1 = bhi(gp);
      const unsigned sp = pk2(g0 * __builtin_amdgcn_rcpf(1.0f + __builtin_amdgcn_exp2f(-1.4426950408889634f * g0)), g1 * __builtin_amdgcn_rcpf(1.0f + __builtin_amdgcn_exp2f(-1.4426950408889634f * g1)));
      const unsigned hp = pk2(blo(sp) * blo(up), bhi(sp) * bhi(up));
      lds_st<u16>(L + HS + (2 * k) * 144 + lane * 2, (u16)hp);
      lds_st<u16>(L + HS + (2 * k + 1) * 144 + lane * 2, (u16)(hp >> 16));
    }
    return true;
  };
  if (w == 0) {
    lds_st<unsigned>(L + CNT, 0u);
#pragma unroll
    for (int i = 0; i < 4; i++) { const unsigned tg = (lane < 36 && r9g != (i < 2 ? 0 : 8)) ? ~e : e; lds_st<v4u>(L + GA + i * 1024 + lane * 16, v4u{0u, tg, 0u, tg}); }
  }
  if (w == 0) {
    const unsigned o = WS_HD + (((lane & 7) >> 1) * 32 + 16 * (lane & 1) + yi) * 4096 + ((h + 16 * (lane & 1) + yi) & 31) * 128 + 12;
#pragma unroll 1
    for (int spin = 0; spin < SPIN_CAP; spin++) {
      asm volatile("" ::: "memory");
      unsigned t_ = ~e;
      if (lane < 8) t_ = ld32<AUX_SC1>(rws, o, 0);
      if (__builtin_popcountll(__ballot(t_ == e)) >= 8) break;
      __builtin_amdgcn_s_sleep(2);
    }
  }
  bar();
  unsigned a_cl = L0 + CL + (lane & 3) * 272 + g * 16, a_cx = L0 + CTX + g * 272 + (16 * v + (lane & 15)) * 2, a_hs = L0 + HS + (lane & 3) * 144 + g * 16;
  unsigned a_h5 = WS_H5 + (8 * w + (lane >> 3)) * 16384 + yj * 128 + cn * 16;
  asm volatile("" : "+v"(a_cl), "+v"(a_cx), "+v"(a_hs), "+v"(a_h5));
  if (w > 0) {
    const int b = v >> 1, ln = 2 * qd + (v & 1), pp = lane >> 3;
    const unsigned po = WS_O3 + ((b * 32 + pp) * 32 + h) * 1024 + ln * 128 + cn * 16, ho = WS_HD + (b * 32 + 8 * cn + pp) * 4096 + ((h + 8 * cn + pp) & 31) * 128 + ln * 16;
    v4u pv[4], hv = v4u{0u, 0u, 0u, ~e};
    if (cn < 4) asm volatile("buffer_load_dwordx4 %0, %1, %2, 0 offen sc1" : "+v"(hv) : "v"(ho), "s"(rws));
    asm volatile("s_barrier" ::: "memory");
#pragma unroll
    for (int i = 0; i < 4; i++) asm volatile("buffer_load_dwordx4 %0, %1, %2, %3 offen sc1" : "=v"(pv[i]) : "v"(po), "s"(rws), "s"(i * 8 * 32768));
    wo_frags();
    unsigned w01, w23, hx;
    float inv;
    auto weights = [&]() {
      const float m = x32_vmax(x16_vmax(max_r8(max_hm(max_x2(max_x1(cn < 4 ? u2f(hv.y) : -INFINITY))))));
      unsigned w0 = pk2(__builtin_amdgcn_exp2f(u2f(hv.y) - m), 0.f);
      w0 = cn < 4 ? w0 : 0u;
      inv = __builtin_amdgcn_rcpf(wave_sum_f(blo(w0) * u2f(hv.z)));
      w0 |= dppu<0x114>(w0);
      w01 = dppu<0x00>(w0) | (dppu<0x55>(w0) << 16); w23 = dppu<0xAA>(w0) | (dppu<0xFF>(w0) << 16);
      hx = hv.x - hv.y - hv.z - KE * e;
      asm volatile("" : "+v"(w01), "+v"(w23), "+v"(inv), "+v"(hx));
    };
    asm volatile("s_waitcnt vmcnt(4)" : "+v"(hv));
    weights();
    asm volatile("s_waitcnt vmcnt(0)" : "+v"(pv[0]), "+v"(pv[1]), "+v"(pv[2]), "+v"(pv[3]));
#pragma unroll 1
    for (int spin = 0; spin < SPIN_CAP; spin++) {
      unsigned cs[4];
#pragma unroll
      for (int i = 0; i < 4; i++) cs[i] = (pv[i].x + pv[i].y) + (pv[i].z + pv[i].w);
      const unsigned c0 = ((cn & 1) ? cs[1] : cs[0]) + dppu<0xB1>((cn & 1) ? cs[0] : cs[1]), c1 = ((cn & 1) ? cs[3] : cs[2]) + dppu<0xB1>((cn & 1) ? cs[2] : cs[3]);
      unsigned c = ((cn & 2) ? c1 : c0) + dppu<0x4E>((cn & 2) ? c0 : c1);
      c += dppu<0x104>(c);
      const bool bad = cn < 4 && (hv.w != e || c != hx);
      if (!__any(bad)) break;
      unsigned need = bad ? 1u << cn : 0u;
      need |= dppu<0xB1>(need); need |= dppu<0x4E>(need); need |= dppu<0x141>(need);
      __builtin_amdgcn_s_sleep(1);
      asm volatile("" ::: "memory");
      if (bad) hv = ld128<AUX_SC1>(rws, ho, 0);
#pragma unroll
      for (int i = 0; i < 4; i++) if ((need >> i) & 1u) pv[i] = ld128<AUX_SC1>(rws, po, i * 8 * 32768);
      weights();
    }
    if (lane == 0) lds_st<unsigned>(L + CNT, 1u);
    float a[8];
#pragma unroll
    for (int d = 0; d < 4; d++) {
      a[d] = dot2(__builtin_amdgcn_perm(pv[3][d], pv[2][d], 0x05040100u), w23, dot2(__builtin_amdgcn_perm(pv[1][d], pv[0][d], 0x05040100u), w01, 0.f));
      a[4 + d] = dot2(__builtin_amdgcn_perm(pv[3][d], pv[2][d], 0x07060302u), w23, dot2(__builtin_amdgcn_perm(pv[1][d], pv[0][d], 0x07060302u), w01, 0.f));
    }
    float u[4], z0, z1;
#pragma unroll
    for (int r = 0; r < 4; r++) { auto s = __builtin_amdgcn_permlane16_swap(f2u(a[r]), f2u(a[r + 4]), false, false); u[r] = u2f(s[0]) + u2f(s[1]); }
    { auto s = __builtin_amdgcn_permlane32_swap(f2u(u[0]), f2u(u[2]), false, false); z0 = u2f(s[0]) + u2f(s[1]); }
    { auto s = __builtin_amdgcn_permlane32_swap(f2u(u[1]), f2u(u[3]), false, false); z1 = u2f(s[0]) + u2f(s[1]); }
    z0 = (z0 + u2f(dppu<0x128>(f2u(z0)))) * inv; z1 = (z1 + u2f(dppu<0x128>(f2u(z1)))) * inv;
    lds_st<unsigned>(L + CL + b * 272 + (64 * (v & 1) + 4 * cn + 32 * (g & 1) + 2 * (g >> 1)) * 2, pk2(z0, z1));
    w2_dma();
  }
  else {
    asm volatile("s_barrier" ::: "memory");
    VMWAIT(0); wo_frags(); w2_dma();
#pragma unroll 1
    for (int spin = 0; spin < (SPIN_CAP << 6); spin++) {
      if (__builtin_amdgcn_readfirstlane(lds_ld<unsigned>(L + CNT)) != 0u) break;
      __builtin_amdgcn_s_sleep(1);
      asm volatile("" ::: "memory");
    }
    gu_issue();
  }
  bar();
  if (w > 0) {
    v4u a[4];
#pragma unroll
    for (int ks = 0; ks < 4; ks++) a[ks] = lds_ld<v4u>(lp(a_cl) + ks * 64);
    sched_fence();
    f4 d0 = mfma32(a[0], fuv[0], f4{0.f, 0.f, 0.f, 0.f}), d1 = mfma32(a[1], fuv[1], f4{0.f, 0.f, 0.f, 0.f});
    d0 = mfma32(a[2], fuv[2], d0); d1 = mfma32(a[3], fuv[3], d1);
    const float cv = sel4(d0 + d1, g);
    lds_st<u16>(lp(a_cx), (u16)pk2(cv, 0.f));
  }
  bar();
  f4 acc[4];
  {
    v4u a[4];
#pragma unroll
    for (int ks = 0; ks < 4; ks++) a[ks] = lds_ld<v4u>(lp(a_cl) + (CTX - CL) + ks * 64);
    sched_fence();
    auto wo_mfma = [&]() {
#pragma unroll
      for (int c = 0; c < 4; c++) acc[c] = mfma32(a[0], fo[c], f4{0.f, 0.f, 0.f, 0.f});
#pragma unroll
      for (int ks = 1; ks < 4; ks++)
#pragma unroll
        for (int c = 0; c < 4; c++) acc[c] = mfma32(a[ks], fo[4 * ks + c], acc[c]);
      sched_fence();
    };
    wo_mfma();
    if (w == 0) {
#pragma unroll 1
      for (int spin = 0; spin < SPIN_CAP; spin++) { if (gu_finish()) break; gu_issue(); }
    }
    VMWAIT(0); w2_frags();
    bar();
    const v4u h0 = lds_ld<v4u>(lp(a_hs)), h1 = lds_ld<v4u>(lp(a_hs) + 64);
#pragma unroll
    for (int c = 0; c < 4; c++) acc[c] = mfma32(h0, f2[c], acc[c]);
#pragma unroll
    for (int c = 0; c < 4; c++) acc[c] = mfma32(h1, f2[4 + c], acc[c]);
    const f4 sx = (g & 1) ? acc[1] : acc[0], sy = (g & 1) ? acc[3] : acc[2], s = (g & 2) ? sy : sx;
    st128(v4u{pk2(s.x, s.y), e, pk2(s.z, s.w), e}, rws, a_h5, 0);
  }
}

DI void role_x(const Args& p, lptr L, int xcd, int xi, int w, int lane, unsigned e) {
  const int t = threadIdx.x;
  const int b = xcd >> 1, s = 16 * (xcd & 1) + xi, fi = 8 * xi + xcd, fk = fi & 7;
  const int g = lane >> 4, rs = (lane >> 3) & 1, cn = lane & 7;
  const int v = (w - 1) & 7;
  const rsrc_t rws = mk(p.ws, WS_END);
  constexpr int QR = 131072, XS = QR + 18432, PS = QR, LS = QR + 8704, HDR = QR + 10240, MX = QR + 11264, ML = QR + 11776, RED = 65536;
  const int fq = 9 * fk + w, fsl = 9 * (fi >> 3) + (fq >> 3), frow = 72 * (fq & 7);
  v2u fa0 = lds_ld<v2u>(L + XS + aoff(1152, lane) + frow * 2), fa1 = lds_ld<v2u>(L + XS + aoff(1152, lane) + frow * 2 + 64);
  unsigned fa2 = lds_ld<u16>(L + XS + aoff_row(1152, lane) + frow * 2 + 128);
  asm volatile("" : "+v"(fa0), "+v"(fa1), "+v"(fa2));
  bar();
  const unsigned L0 = (unsigned)(size_t)L;
  const unsigned hqo = 8 * b * SP + WS_QH + (16 * v + (lane & 15)) * 8;
  v2u hq = v2u{0u, 0u};
#define QDMA(i) DMA(L0 + QR + (4 * v + (i)) * 1024, 2 * g * SP + ((lane & 15) ^ (((i) << 2) | (v & 3))) * 16, rws, (8 * b + ((i) & 1)) * SP + WS_QL + (2 * v + ((i) >> 1)) * 1024, "sc1")
  if (w > 0) {
    QDMA(0); QDMA(1); QDMA(2); QDMA(3);
    asm volatile("buffer_load_dwordx2 %0, %1, %2, 0 offen sc1" : "=v"(hq) : "v"(hqo), "s"(rws));
  }
  const int r32 = lane & 31, hf = lane >> 5;
  const bool qkw = w > 0 && w <= 4;
  unsigned ta[4], tb[4], ca[8], qa[8];
  {
    const int q = (lane & 15) >> 2, pq = lane & 3;
#pragma unroll
    for (int i = 0; i < 4; i++) {
      const int ct = i >> 1, tt = i & 1;
      ta[i] = L0 + (8 * hf + 4 * tt + q) * 1024 + (((8 * v + 4 * ct + 2 * ((lane >> 4) & 1) + (pq >> 1)) ^ ((q << 2) | ((2 * hf + tt) & 3))) * 16) + 8 * (pq & 1);
      tb[i] = ta[i] + 65536;
      asm volatile("" : "+v"(ta[i]), "+v"(tb[i]));
    }
    const int ksw = ((r32 & 3) << 2) | ((r32 >> 2) & 3);
#pragma unroll
    for (int k = 0; k < 8; k++) {
      qa[k] = L0 + QR + r32 * 1024 + (((2 * k + hf) ^ ksw) << 4);
      ca[k] = qa[k] - QR + (v & 3) * 32768;
      asm volatile("" : "+v"(ca[k]), "+v"(qa[k]));
    }
  }
  const rsrc_t rf = mk((const char*)(fsl < 72 ? p.w1 : p.w3) + (size_t)frow * 9216 + (fsl < 72 ? fsl : fsl - 72) * 128, 72 * 9216);
  unsigned fvo = (16 * rs + 4 * g) * 9216 + cn * 16;
  asm volatile("" : "+v"(fvo));
  auto q_sum_ok = [&]() {
    unsigned cs = lane < 16 ? 0u - hq.x : 0u;
#pragma unroll
    for (int i = 0; i < 4; i++) { const v4u q = lds_ld<v4u>(L + QR + (4 * v + i) * 1024 + lane * 16); cs += (q.x + q.y) + q.z; cs += q.w; }
    return __all(hq.y == e) && __builtin_amdgcn_readfirstlane(wave_sum_u(cs)) + 16u * KE * e == 0u;
  };
  if (w > 0) {
    asm volatile("s_waitcnt vmcnt(0)" : "+v"(hq));
#pragma unroll 1
    for (int spin = 0; spin < SPIN_CAP; spin++) {
      if (q_sum_ok()) break;
#pragma unroll 1
      for (; spin < SPIN_CAP; spin++) {
        asm volatile("" ::: "memory");
        hq = ld64<AUX_SC1>(rws, hqo, 0);
        if (__all(hq.y == e)) break;
        __builtin_amdgcn_s_sleep(2);
      }
      QDMA(0); QDMA(1); QDMA(2); QDMA(3);
      VMWAIT(0);
    }
  }
  bar();
  v4u fb0[4], fb1[4], fb2, cb[16];
#define FLOAD(n) do { if ((n) < 4) fb0[(n) & 3] = ld128<AUX_NT>(rf, fvo, ((n) & 3) * 9216); else if ((n) < 8) fb1[(n) & 3] = ld128<AUX_NT>(rf, fvo, (32 + ((n) & 3)) * 9216); \
                      else fb2 = ld128<AUX_NT>(rf, (64 + 2 * g + rs) * 9216 + cn * 16, 0); } while (0)
  auto load_cb = [&]() {
#pragma unroll
    for (int ks = 0; ks < 8; ks++)
#pragma unroll
      for (int ct = 0; ct < 2; ct++)
        cb[2 * ks + ct] = trfrag(lp(ks < 4 ? ta[2 * ct] : tb[2 * ct]) + (ks & 3) * 16384, lp(ks < 4 ? ta[2 * ct + 1] : tb[2 * ct + 1]) + (ks & 3) * 16384);
  };
  f16v sa;
  float tm = 0.f;
  if (qkw) {
    v4u qf[QD], cr[QD];
#pragma unroll
    for (int i = 0; i < QD; i++) { qf[i] = lds_ld<v4u>(lp(qa[i])); cr[i] = lds_ld<v4u>(lp(ca[i])); }
    sched_fence();
#pragma unroll
    for (int i = 0; i < 16; i++) sa[i] = 0.f;
#pragma unroll
    for (int ks = 0; ks < 32; ks++) {
      sa = mfmaT(cr[ks % QD], qf[ks % QD], sa);
      if (ks + QD < 32) {
        qf[ks % QD] = lds_ld<v4u>(lp(qa[(ks + QD) & 7]) + ((ks + QD) >> 3) * 256);
        cr[ks % QD] = lds_ld<v4u>(lp(ca[(ks + QD) & 7]) + ((ks + QD) >> 3) * 256);
      } else {
#pragma unroll
        for (int j = (ks + QD - 32) * 16 / QD; j < (ks + QD - 31) * 16 / QD; j++)
          cb[j] = trfrag(lp(j < 8 ? ta[2 * (j & 1)] : tb[2 * (j & 1)]) + ((j >> 1) & 3) * 16384, lp(j < 8 ? ta[2 * (j & 1) + 1] : tb[2 * (j & 1) + 1]) + ((j >> 1) & 3) * 16384);
      }
      sched_fence();
    }
    asm volatile("s_nop 7\n\ts_nop 7" : "+v"(sa));
    const float t0_ = vmax3(vmax3(sa[0], sa[1], sa[2]), sa[3], sa[4]), t1_ = vmax3(vmax3(sa[5], sa[6], sa[7]), sa[8], sa[9]), t2_ = vmax3(vmax3(sa[10], sa[11], sa[12]), sa[13], sa[14]);
    tm = x32_vmax(vmax(vmax3(t0_, t1_, t2_), sa[15]));
  } else {
#pragma unroll
    for (int n = 0; n < 9; n++) FLOAD(n);
    sched_fence();
    if (w > 0) load_cb();
  }
  sched_fence();
  asm volatile("s_barrier" ::: "memory");
  if (qkw) lds_st<float>(L + MX + v * 128 + r32 * 4, tm);
  bar();
  if (qkw) {
    const float mrow = vmax(vmax3(lds_ld<float>(L + MX + r32 * 4), lds_ld<float>(L + MX + 128 + r32 * 4), lds_ld<float>(L + MX + 256 + r32 * 4)), lds_ld<float>(L + MX + 384 + r32 * 4));
    const float us = (e & 1u) ? 0.5f : 1.0f, mo = -mrow * us, ps = (e & 1u) ? 2.0f : 1.0f;
    f2 l2 = {0.f, 0.f};
#pragma unroll
    for (int i = 0; i < 4; i++) {
      const f2 pa = f2{__builtin_amdgcn_exp2f(fmaf(sa[4 * i], us, mo)), __builtin_amdgcn_exp2f(fmaf(sa[4 * i + 1], us, mo))} * ps;
      const f2 pb = f2{__builtin_amdgcn_exp2f(fmaf(sa[4 * i + 2], us, mo)), __builtin_amdgcn_exp2f(fmaf(sa[4 * i + 3], us, mo))} * ps;
      lds_st<v2u>(L + PS + r32 * 272 + (32 * v + 8 * i + 4 * hf) * 2, v2u{pk2(pa.x, pa.y), pk2(pb.x, pb.y)});
      l2 += pa + pb;
    }
    lds_st<float>(L + LS + (2 * v + hf) * 128 + r32 * 4, l2.x + l2.y);
  }
  bar();
  if (w > 0) {
    f16v o0, o1;
    v4u pa[PAD];
    const lptr pp = L + PS + r32 * 272 + 16 * hf;
#pragma unroll
    for (int ks = 0; ks < PAD; ks++) pa[ks] = lds_ld<v4u>(pp + 32 * ks);
    sched_fence();
#pragma unroll
    for (int i = 0; i < 16; i++) { o0[i] = 0.f; o1[i] = 0.f; }
#pragma unroll
    for (int ks = 0; ks < 8; ks++) {
      o0 = mfmaT(pa[ks % PAD], cb[2 * ks], o0);
      o1 = mfmaT(pa[ks % PAD], cb[2 * ks + 1], o1);
      if (ks + PAD < 8) pa[ks % PAD] = lds_ld<v4u>(pp + 32 * (ks + PAD));
      sched_fence();
    }
    asm volatile("" : "+v"(fb0[0]), "+v"(fb0[1]), "+v"(fb0[2]), "+v"(fb0[3]), "+v"(fb1[0]), "+v"(fb1[1]), "+v"(fb1[2]), "+v"(fb1[3]), "+v"(fb2));
    unsigned pk[16];
    const unsigned ob = WS_O3 + ((b * 32 + s) * 32 + 4 * hf) * 1024 + v * 128 + r32 * 4;
#pragma unroll
    for (int r = 0; r < 16; r++) { pk[r] = pk2(o0[r], o1[r]); st32(pk[r], rws, ob, ((r & 3) + 8 * (r >> 2)) * 1024); }
    sched_fence();
    unsigned c8[8], c4[4], c2[2];
#pragma unroll
    for (int r = 0; r < 8; r++) { auto sw = __builtin_amdgcn_permlane16_swap(pk[r], pk[r + 8], false, false); c8[r] = sw[0] + sw[1]; }
#pragma unroll
    for (int r = 0; r < 4; r++) c4[r] = ((lane & 8) ? c8[r + 4] : c8[r]) + dppu<0x128>((lane & 8) ? c8[r] : c8[r + 4]);
#pragma unroll
    for (int r = 0; r < 2; r++) c2[r] = ((lane & 4) ? c4[r + 2] : c4[r]) + dppu<0x141>((lane & 4) ? c4[r] : c4[r + 2]);
    unsigned c1 = ((lane & 2) ? c2[1] : c2[0]) + dppu<0x4E>((lane & 2) ? c2[0] : c2[1]);
    c1 += dppu<0xB1>(c1);
    lds_st<unsigned>(L + HDR + v * 128 + (((lane >> 1) & 3) + 8 * ((lane >> 3) & 3) + 4 * hf) * 4, c1);
  } else {
    asm volatile("" : "+v"(fb0[0]), "+v"(fb0[1]), "+v"(fb0[2]), "+v"(fb0[3]), "+v"(fb1[0]), "+v"(fb1[1]), "+v"(fb1[2]), "+v"(fb1[3]), "+v"(fb2));
    const float hm = vmax(vmax3(lds_ld<float>(L + MX + r32 * 4), lds_ld<float>(L + MX + 128 + r32 * 4), lds_ld<float>(L + MX + 256 + r32 * 4)), lds_ld<float>(L + MX + 384 + r32 * 4)) * ((e & 1u) ? 0.5f : 1.0f);
    float hl = 0.f;
#pragma unroll
    for (int i = 0; i < 8; i++) hl += lds_ld<float>(L + LS + i * 128 + r32 * 4);
    lds_st<v4u>(L + ML + r32 * 16, v4u{f2u(hm) + f2u(hl) + KE * e, f2u(hm), f2u(hl), e});
  }
  bar();
  if (qkw) {
    const int tq = t - 64;
    v4u hd = lds_ld<v4u>(L + ML + (tq >> 3) * 16);
    hd.x += lds_ld<unsigned>(L + HDR + cn * 128 + (tq >> 3) * 4);
    st128(hd, rws, WS_HD + (b * 32 + s) * 4096 + ((tq * 16 + s * 128) & 4095), 0);
  }
  sched_fence();
  auto ffn_unit = [&]() {
    f4 acc[4];
#pragma unroll
    for (int j = 0; j < 4; j++) acc[j] = f4{0.f, 0.f, 0.f, 0.f};
    v2u f[8];
    blk_transpose(fb0, f); frag_mfma(f, fa0, acc, lane);
    blk_transpose(fb1, f); frag_mfma(f, fa1, acc, lane);
    row_split(fb2, f); frag_mfma(f, v2u{fa2, 0u}, acc, lane);
    red_store(L + RED + w * 2048, acc, lane);
  };
  if (qkw) {
#pragma unroll
    for (int n = 0; n < 9; n++) FLOAD(n);
    sched_fence();
  } else ffn_unit();
  bar();
  f4 sm = {0.f, 0.f, 0.f, 0.f};
  const int seg = w - 5;
  const lptr rq = L + RED + cpos(lane) * 16;
  if (qkw) ffn_unit();
  else if (w == 5 || w == 6) {
#pragma unroll
    for (int k = 0; k < 9; k++) {
      if (k >= 1 && k <= 4) continue;
      const f4 u = lds_ld<f4>(rq + k * 2048) + lds_ld<f4>(rq + k * 2048 + 1024);
      if ((k >= 8 - fk) == (seg == 1)) sm += u;
    }
    asm volatile("" : "+v"(sm));
  }
  bar();
  if (w == 5 || w == 6) {
#pragma unroll
    for (int k = 1; k <= 4; k++) {
      const f4 u = lds_ld<f4>(rq + k * 2048) + lds_ld<f4>(rq + k * 2048 + 1024);
      if ((k >= 8 - fk) == (seg == 1)) sm += u;
    }
    st128(v4u{pk2(sm.x, sm.y), e, pk2(sm.z, sm.w), e}, rws, WS_GU + ((1 - seg) * 9216 + (9 * (fi >> 3) + fk + seg) * 64 + lane) * 16, 0);
  }
  if (fi < 72) {
    constexpr int FIN = 4096;
    const rsrc_t rx = mk(p.x, 4608);
    unsigned xr = 0;
    if (w == 8) xr = __builtin_amdgcn_raw_buffer_load_b16(rx, lane < 32 ? (lane >> 3) * 1152 + (8 * fi + (lane & 7)) * 2 : OOB, 0, 0);
    if (w == 0) {
      const unsigned o = WS_H5 + fi * 16384 + (((lane & 7) * 17 + fi) & 127) * 128 + 4;
#pragma unroll 1
      for (int spin = 0; spin < SPIN_CAP; spin++) {
        asm volatile("" ::: "memory");
        unsigned t_ = ~e;
        if (lane < 8) t_ = ld32<AUX_SC1>(rws, o, 0);
        if (__builtin_popcountll(__ballot(t_ == e)) >= 4) break;
        __builtin_amdgcn_s_sleep(2);
      }
    }
    bar();
    if (w < 8) {
      v4u a0, a1;
      a0.y = a0.w = a1.y = a1.w = ~e;
      const unsigned o = WS_H5 + fi * 16384 + w * 2048 + lane * 16;
#pragma unroll 1
      for (int spin = 0; spin < SPIN_CAP; spin++) {
        asm volatile("" ::: "memory");
        const bool k0 = (a0.y == e) & (a0.w == e), k1 = (a1.y == e) & (a1.w == e);
        if (!k0) a0 = ld128<AUX_SC1>(rws, o, 0);
        if (!k1) a1 = ld128<AUX_SC1>(rws, o, 1024);
        const bool ok = (a0.y == e) & (a0.w == e) & (a1.y == e) & (a1.w == e);
        if (__all(ok)) break;
      }
      f4 u = f4{blo(a0.x) + blo(a1.x), bhi(a0.x) + bhi(a1.x), blo(a0.z) + blo(a1.z), bhi(a0.z) + bhi(a1.z)};
      auto s0 = __builtin_amdgcn_permlane16_swap(f2u(u.x), f2u(u.y), false, false), s1 = __builtin_amdgcn_permlane16_swap(f2u(u.z), f2u(u.w), false, false);
      auto s2 = __builtin_amdgcn_permlane32_swap(f2u(u2f(s0[0]) + u2f(s0[1])), f2u(u2f(s1[0]) + u2f(s1[1])), false, false);
      float z = u2f(s2[0]) + u2f(s2[1]);
      z += u2f(dppu<0x128>(f2u(z)));
      if (rs == 0) lds_st<float>(L + FIN + w * 128 + (g * 8 + cn) * 4, z);
    }
    bar();
    if (w == 8 && lane < 32) {
      float a = 0.f;
#pragma unroll
      for (int k = 0; k < 8; k++) a += lds_ld<float>(L + FIN + k * 128 + lane * 4);
      ((u16*)p.out)[(lane >> 3) * 576 + 8 * fi + (lane & 7)] = (u16)pk2(blo(xr) + a, 0.f);
      if (fi == 0 && lane == 0) st32(e, rws, WS_E, 0);
    }
  }
}

__global__ void __launch_bounds__(NT, 3) mla_kernel(Args p) {
#if defined(__HIP_DEVICE_COMPILE__)
  extern __shared__ __attribute__((aligned(16))) char smem[];
  const lptr L = (lptr)smem;
  const int w = __builtin_amdgcn_readfirstlane(threadIdx.x >> 6), lane = threadIdx.x & 63;
  const int xcd = blockIdx.x & 7, r = blockIdx.x >> 3;
  const int g = lane >> 4, cn = lane & 7;
  const bool isx = r >= 16;
  const unsigned L0 = (unsigned)(size_t)L, LA = L0 + (isx ? 131072 : 0);
  const int mq = xcd >> 1, dh = (xcd ^ (r >> 3)) & 1;
  unsigned pfx = 0;
  v2u xv[3];
  if (w == 8) {
    u64 pc; asm volatile("s_getpc_b64 %0" : "=s"(pc));
    asm volatile("buffer_load_dword %0, %1, %2, 0 offen" : "=v"(pfx) : "v"(lane * 2048 + (r & 15) * 128), "s"(mk((const void*)(pc & ~(u64)255), CODE_BYTES)));
  }
  sched_fence();
  asm volatile("" ::"s"(p.x), "s"(p.w3), "s"(p.w2), "s"(p.out));
  if (w < 4) {
    const rsrc_t rx = mk(p.x, 4608);
    const int xs_ = w * 1152;
    asm volatile("buffer_load_dwordx2 %0, %1, %2, %3 offen" : "=v"(xv[0]) : "v"(lane * 8), "s"(rx), "s"(xs_));
    asm volatile("buffer_load_dwordx2 %0, %1, %2, %3 offen offset:512" : "=v"(xv[1]) : "v"(lane * 8), "s"(rx), "s"(xs_));
    asm volatile("buffer_load_dwordx2 %0, %1, %2, %3 offen offset:1024" : "=v"(xv[2]) : "v"(lane < 16 ? lane * 8 : OOB), "s"(rx), "s"(xs_));
  }
  unsigned E;
  asm volatile("s_load_dword %0, %1, 0x0" : "=s"(E) : "s"(p.ws));
  {
    const rsrc_t r1 = mk(p.wq, 576 * 8192);
    const unsigned vo = (lane >> 3) * 8192 + ((cn ^ (2 * (g & 1))) << 4);
    const int so = (144 * mq + 16 * w) * 8192 + r * 256 + dh * 128;
    DMA(LA + w * 2048, vo, r1, so, "nt");
    DMA(LA + w * 2048 + 1024, vo ^ 64, r1, so + 8 * 8192, "nt");
  }
  asm volatile("s_barrier" ::: "memory");
  const rsrc_t rc = mk(p.ckv, 4 * 4096 * 1024);
  const int crow0 = ((xcd >> 1) * 4096 + 128 * (16 * (xcd & 1) + (r & 15)) + 16 * ((w - 1) & 7)) << 10;
  const unsigned lb = L0 + ((w - 1) & 7) * 16384;
#define CDMA(i) DMA(lb + (i) * 1024, (lane ^ ((((i) & 3) << 2) | ((i) >> 2))) * 16, rc, crow0 + (i) * 1024, "nt")
  unsigned tch = 0;
  if (w == 0) VMWAIT(2);
  else if (isx) {
    if (w > 6) {
      asm volatile("buffer_load_dword %0, %1, %2, %3 offen" : "=v"(tch) : "v"((lane >> 1) * 8192 + (lane & 1) * 128), "s"(mk(p.wuk, 512 * 8192)), "s"((128 * (r & 3) + 32 * (w - 5)) * 8192 + (4 * xcd + ((r & 15) >> 2)) * 256));
    }
    CDMA(0);
    if (w < 4) VMWAIT(3);
  } else {
    const rsrc_t r2 = mk(p.wuk, 512 * 8192);
    const int so = (128 * (r & 3) + 16 * (w - 1)) * 8192 + (4 * xcd + (r >> 2)) * 256;
#pragma unroll
    for (int i = 0; i < 4; i++) DMA(L0 + 32768 + (w - 1) * 4096 + i * 1024, g * 8192 + (((lane & 15) ^ ((g << 2) | i)) << 4), r2, so + i * 32768, "nt");
    if (w < 4) VMWAIT(6);
  }
  asm volatile("" : "+v"(xv[0]), "+v"(xv[1]), "+v"(xv[2]));
  if (w < 4) layer_norm_row(xv, lp(LA + 18432 + w * 1152), lane);
  asm volatile("s_waitcnt lgkmcnt(0)\n\ts_barrier" : "+s"(E)::"memory");
  const unsigned e = E + 1u;
  {
    v4u xa[4]; v2u xl;
    const lptr xp = lp(LA + 18432 + (lane & 3) * 1152 + 288 * mq);
#pragma unroll
    for (int ks = 0; ks < 4; ks++) xa[ks] = lds_ld<v4u>(xp + g * 16 + ks * 64);
    xl = lds_ld<v2u>(xp + 256 + g * 8);
    const int q = (lane & 15) >> 2, pq = lane & 3, c = (w - 1) & 3;
    unsigned t0 = LA + (8 * g + q) * 128 + ((2 * (c ^ ((q >> 1) | ((g & 1) << 1))) + (pq >> 1)) << 4) + 8 * (pq & 1);
    unsigned t1 = LA + (128 + 4 * g + q) * 128 + ((2 * (c ^ ((q >> 1) | (g & 2))) + (pq >> 1)) << 4) + 8 * (pq & 1);
    unsigned ho = lane < 16 ? (4 * (2 * mq + dh) + c) * SP + WS_H1 + (1 + r) * 1024 + lane * 16 : OOB;
    asm volatile("" : "+v"(xa[0]), "+v"(xa[1]), "+v"(xa[2]), "+v"(xa[3]), "+v"(xl), "+v"(t0), "+v"(t1), "+v"(ho));
    if (w == 0) VMWAIT(0); else if (!isx) VMWAIT(4); else if (w > 6) VMWAIT(2); else VMWAIT(1);
    asm volatile("" : "+v"(pfx));
    asm volatile("s_barrier" ::: "memory");
    if (w >= 1 && w <= 4) {
      v4u bf[4];
#pragma unroll
      for (int ks = 0; ks < 4; ks++) bf[ks] = trfrag(lp(t0) + ks * 4096, lp(t0) + ks * 4096 + 512);
      const v2u bl = __builtin_bit_cast(v2u, __builtin_amdgcn_ds_read_tr16_b64_v4i16((LAS s4*)lp(t1)));
      sched_fence();
      f4 d0 = mfma32(xa[0], bf[0], f4{0.f, 0.f, 0.f, 0.f}), d1 = mfma32(xa[1], bf[1], f4{0.f, 0.f, 0.f, 0.f});
      d0 = mfma32(xa[2], bf[2], d0); d1 = mfma32(xa[3], bf[3], d1);
      d0 = mfma16(xl, bl, d0);
      d0 += d1;
      st128(v4u{pk2(d0.x, d0.y), e, pk2(d0.z, d0.w), e}, mk(p.ws, WS_END), ho, 0);
    }
  }
  if (isx && w > 0) {
#pragma unroll 1
    for (int k = 1; k < 16; k++) {
      CDMA(k);
      VMWAIT(3);
      asm volatile("" : "+v"(tch));
    }
  }
  int r2 = r;
  asm volatile("" : "+s"(r2));
  if (r2 >= 16) { role_x(p, L, xcd, r2 - 16, w, lane, e); __builtin_amdgcn_endpgm(); }
  role_y(p, L, xcd, r2, w, lane, e);
#endif
}
}

extern "C" void kernel_launch(void* const* d_in, const int* in_sizes, int n_in, void* d_out, int out_size, void* d_ws, size_t ws_size, hipStream_t stream) {
  mla::Args a;
  a.x = d_in[0]; a.ckv = d_in[1]; a.wq = d_in[2]; a.wuk = d_in[3]; a.wuv = d_in[4]; a.wo = d_in[5];
  a.w1 = d_in[7]; a.w3 = d_in[8]; a.w2 = d_in[9]; a.out = d_out; a.ws = d_ws;
  (void)hipFuncSetAttribute((const void*)mla::mla_kernel, hipFuncAttributeMaxDynamicSharedMemorySize, mla::LDS_BYTES);
  hipLaunchKernelGGL(mla::mla_kernel, dim3(256), dim3(mla::NT), mla::LDS_BYTES, stream, a);
}
```
